```python
import torch

def _softmax(x, axis=-1):
    return torch.softmax(x, dim=axis)

def _gelu(x):
    return torch.nn.functional.gelu(x, approximate='tanh')

def _silu(x):
    return torch.nn.functional.silu(x)

def _sigmoid(x):
    return torch.sigmoid(x)

def _layernorm(x, axis=-1, eps=1e-05):
    m = x.mean(dim=axis, keepdim=True)
    v = x.var(dim=axis, keepdim=True, unbiased=False)
    return (x - m) / torch.sqrt(v + eps)

def _scatter_add(tgt, idx, src, axis=0):
    return tgt.clone().index_add_(axis, idx, src)

def _segment_sum(src, seg, n):
    out = torch.zeros((n,) + src.shape[1:], dtype=src.dtype)
    return out.index_add_(0, seg, src)

def _pad(x, pw):
    flat = tuple((v for pair in reversed(pw) for v in pair))
    return torch.nn.functional.pad(x, flat)
kernel_name = 'adaLN_cond__compressed_kv_cla__geglu_ffn__prompt'

def setup_inputs():

    _w = lambda a: (lambda t: t.to(torch.bfloat16) if t.is_floating_point() else t)(torch.as_tensor(a))

    def _A_setup():
        B, H, Sq, Sk, D, M = (1, 32, 2048, 2048, 128, 512)
        Dc = 4 * D
        return {'x': _w((lambda *s: torch.randn(*s, dtype=torch.bfloat16) * 0.05)(B, Sq, M)), 'c_kv': _w((lambda *s: torch.randn(*s, dtype=torch.bfloat16) * 1.6)(B, Sk, Dc)), 'w_q': _w((lambda *s: torch.randn(*s, dtype=torch.bfloat16) * 0.8)(M, H, D) * M ** (-0.5)), 'w_uk': _w((lambda *s: torch.randn(*s, dtype=torch.bfloat16) * 0.8)(Dc, H, D) * Dc ** (-0.5)), 'w_uv': _w((lambda *s: torch.randn(*s, dtype=torch.bfloat16) * 1.6)(Dc, H, D) * Dc ** (-0.5)), 'w_o': _w((lambda *s: torch.randn(*s, dtype=torch.bfloat16) * 1.6)(H, D, M) * (H * D) ** (-0.5))}
    _A_in = _A_setup()

    def _B_setup():
        B, S, D, F = (1, 2048, 512, 1792)
        return {'x': _w((lambda *s: torch.randn(*s, dtype=torch.bfloat16) * 0.05)(B, S, D)), 'w_gate': _w((lambda *s: torch.randn(*s, dtype=torch.bfloat16) * 0.05)(D, F)), 'w_up': _w((lambda *s: torch.randn(*s, dtype=torch.bfloat16) * 0.05)(D, F)), 'w_down': _w((lambda *s: torch.randn(*s, dtype=torch.bfloat16) * 0.05)(F, D))}
    _B_in = _B_setup()
    return {'A_x': _A_in['x'], 'A_c_kv': _A_in['c_kv'], 'A_w_q': _A_in['w_q'], 'A_w_uk': _A_in['w_uk'], 'A_w_uv': _A_in['w_uv'], 'A_w_o': _A_in['w_o'], 'B_x': _B_in['x'], 'B_w_gate': _B_in['w_gate'], 'B_w_up': _B_in['w_up'], 'B_w_down': _B_in['w_down'], 'c': _w((lambda *s: torch.randn(*s, dtype=torch.bfloat16) * 0.1)(1, 512) * 8.0), 'w_mod': _w((lambda *s: torch.randn(*s, dtype=torch.bfloat16) * 0.1)(512, 6 * 512) * 512 ** (-0.5) * 8.0)}

def reference(A_x, A_c_kv, A_w_q, A_w_uk, A_w_uv, A_w_o, B_x, B_w_gate, B_w_up, B_w_down, c, w_mod):

    def _A_ref(x, c_kv, w_q, w_uk, w_uv, w_o):
        B, Sq, M = (x.shape[0], x.shape[1], x.shape[2])
        Sk = c_kv.shape[1]
        H, D = (w_q.shape[1], w_q.shape[2])
        q = torch.einsum('bqm,mhd->bhqd', x, w_q)
        k = torch.einsum('bkc,chd->bhkd', c_kv, w_uk)
        v = torch.einsum('bkc,chd->bhkd', c_kv, w_uv)
        scores = torch.einsum('bhqd,bhkd->bhqk', q, k) * D ** (-0.5)
        off = Sk - Sq
        qi = torch.arange(Sq)[:, None]
        kj = torch.arange(Sk)[None, :]
        cmask = kj <= qi + off
        scores = torch.where(cmask[None, None], scores, -1000000000.0)
        p = _softmax(scores, axis=-1)
        ctx = torch.einsum('bhqk,bhkd->bhqd', p, v)
        return torch.einsum('bhqd,hdm->bqm', ctx, w_o)

    def _B_ref(x, w_gate, w_up, w_down):
        return _gelu(x @ w_gate) * (x @ w_up) @ w_down
    x = A_x
    mod = (_silu(c) @ w_mod).reshape(c.shape[0], 6, 1, -1)
    sa, ca, ga, sb, cb, gb = (mod[:, 0].clone(), mod[:, 1].clone(), mod[:, 2].clone(), mod[:, 3].clone(), mod[:, 4].clone(), mod[:, 5].clone())
    h = _layernorm(x, axis=-1) * (1 + ca) + sa
    x = x + ga * _A_ref(h, A_c_kv, A_w_q, A_w_uk, A_w_uv, A_w_o)
    h = _layernorm(x, axis=-1) * (1 + cb) + sb
    x = x + gb * _B_ref(h, B_w_gate, B_w_up, B_w_down)
    return x
```

```cpp
#include <hip/hip_runtime.h>
#include <hip/hip_bf16.h>
#include <cstdint>
#include <cstdio>

typedef unsigned short u16;
typedef short bf16x8 __attribute__((ext_vector_type(8)));
typedef short s16x4 __attribute__((ext_vector_type(4)));
typedef short v4i16_t __attribute__((ext_vector_type(4)));
typedef float f32x16 __attribute__((ext_vector_type(16)));
typedef float f32x4 __attribute__((ext_vector_type(4)));
typedef unsigned u32x4 __attribute__((ext_vector_type(4)));
#define LAS __attribute__((address_space(3)))
typedef LAS const char* lds_cptr;
typedef LAS char* lds_ptr;
#define DI __device__ __forceinline__
#define SBAR() __builtin_amdgcn_sched_barrier(0)
#define MFMA(a, b, c) __builtin_amdgcn_mfma_f32_32x32x16_bf16((a), (b), (c), 0, 0, 0)

constexpr int SEQ = 2048, DM = 512, NH = 32, HD = 128, FF = 1792;
constexpr float QSCALE = 0.08838834764831845f * 1.4426950408889634f;
constexpr float THR = 8.0f;

constexpr size_t WS_MOD = 0;
constexpr size_t WS_HF  = 65536;
constexpr size_t WS_X1F = WS_HF + (size_t)SEQ * DM * 2;
constexpr size_t WS_H2F = WS_X1F + (size_t)SEQ * DM * 2;
constexpr size_t WS_QF  = WS_H2F + (size_t)SEQ * DM * 2;
constexpr size_t WS_KF  = WS_QF + (size_t)SEQ * NH * HD * 2;
constexpr size_t WS_VF  = WS_KF + (size_t)SEQ * NH * HD * 2;
constexpr size_t WS_CF  = WS_VF + (size_t)SEQ * NH * HD * 2;
constexpr size_t WS_GF  = WS_CF + (size_t)SEQ * NH * HD * 2;
constexpr size_t WS_END = WS_GF + (size_t)SEQ * FF * 2;

DI unsigned cvtpk(float lo, float hi) { unsigned r; asm("v_cvt_pk_bf16_f32 %0, %1, %2" : "=v"(r) : "v"(lo), "v"(hi)); return r; }
DI float bf_lo(unsigned w) { return __uint_as_float(w << 16); }
DI float bf_hi(unsigned w) { return __uint_as_float(w & 0xffff0000u); }
DI float rbf(float x) { return bf_lo(cvtpk(x, x)); }
DI s16x4 trrd(lds_cptr p) { return __builtin_bit_cast(s16x4, __builtin_amdgcn_ds_read_tr16_b64_v4i16((LAS v4i16_t*)p)); }
DI bf16x8 cat8(s16x4 a, s16x4 b) { return __builtin_shufflevector(a, b, 0, 1, 2, 3, 4, 5, 6, 7); }
DI unsigned rfl(unsigned x) { return __builtin_amdgcn_readfirstlane(x); }
DI void dma16(unsigned m0v, unsigned voff, __amdgpu_buffer_rsrc_t srd, unsigned soff) {
  asm volatile("s_mov_b32 m0, %0\n\ts_nop 0\n\tbuffer_load_dwordx4 %1, %2, %3 offen lds" ::"s"(m0v), "v"(voff), "s"(srd), "s"(soff) : "memory");
}
DI __amdgpu_buffer_rsrc_t mk_srd(const void* p, unsigned bytes) { return __builtin_amdgcn_make_buffer_rsrc((void*)p, (short)0, (int)bytes, 0x00020000); }

__global__ __launch_bounds__(256) void k_mod(const u16* __restrict__ c, const u16* __restrict__ wmod, float* __restrict__ mod) {
  __shared__ float red[64][33];
  const int tid = threadIdx.x, cc = tid & 3, rg = tid >> 2;
  const int col0 = blockIdx.x * 32 + cc * 8;
  float acc[8] = {0, 0, 0, 0, 0, 0, 0, 0};
  u32x4 w[8];
#pragma unroll
  for (int i = 0; i < 8; ++i) w[i] = *(const u32x4*)(wmod + (size_t)(rg * 8 + i) * 3072 + col0);
  const u32x4 cw = *(const u32x4*)(c + rg * 8);
#pragma unroll
  for (int i = 0; i < 8; ++i) {
    const float cv = (i & 1) ? bf_hi(cw[i >> 1]) : bf_lo(cw[i >> 1]);
    const float s = rbf(cv / (1.f + __expf(-cv)));
#pragma unroll
    for (int j = 0; j < 4; ++j) { acc[2 * j] += s * bf_lo(w[i][j]); acc[2 * j + 1] += s * bf_hi(w[i][j]); }
  }
#pragma unroll
  for (int j = 0; j < 8; ++j) red[rg][cc * 8 + j] = acc[j];
  __syncthreads();
  if (tid < 32) {
    float s = 0;
    for (int i = 0; i < 64; ++i) s += red[i][tid];
    mod[blockIdx.x * 32 + tid] = rbf(s);
  }
}

template <bool SRC_FRAG>
__global__ __launch_bounds__(256) void k_ln(const u16* __restrict__ x, const float* __restrict__ svec, const float* __restrict__ cvec, u16* __restrict__ outF) {
  const int lane = threadIdx.x & 63, row = blockIdx.x * 4 + (threadIdx.x >> 6);
  const size_t foff = ((size_t)((row >> 5) * 32 + (lane >> 1)) * 64 + (lane & 1) * 32 + (row & 31)) * 8;
  const u32x4 w = SRC_FRAG ? *(const u32x4*)(x + foff) : *(const u32x4*)(x + (size_t)row * DM + lane * 8);
  float v[8];
#pragma unroll
  for (int j = 0; j < 4; ++j) { v[2 * j] = bf_lo(w[j]); v[2 * j + 1] = bf_hi(w[j]); }
  float s = 0;
#pragma unroll
  for (int j = 0; j < 8; ++j) s += v[j];
#pragma unroll
  for (int o = 32; o; o >>= 1) s += __shfl_xor(s, o);
  const float mu = s * (1.f / DM);
  float q = 0;
#pragma unroll
  for (int j = 0; j < 8; ++j) { v[j] -= mu; q += v[j] * v[j]; }
#pragma unroll
  for (int o = 32; o; o >>= 1) q += __shfl_xor(q, o);
  const float rs = rsqrtf(q * (1.f / DM) + 1e-5f);
  const f32x4 s0 = *(const f32x4*)(svec + lane * 8), s1 = *(const f32x4*)(svec + lane * 8 + 4);
  const f32x4 c0 = *(const f32x4*)(cvec + lane * 8), c1 = *(const f32x4*)(cvec + lane * 8 + 4);
  float h[8];
#pragma unroll
  for (int j = 0; j < 4; ++j) { h[j] = v[j] * rs * rbf(1.f + c0[j]) + s0[j]; h[4 + j] = v[4 + j] * rs * rbf(1.f + c1[j]) + s1[j]; }
  u32x4 o = {cvtpk(h[0], h[1]), cvtpk(h[2], h[3]), cvtpk(h[4], h[5]), cvtpk(h[6], h[7])};
  *(u32x4*)(outF + foff) = o;
}

enum { EPI_K = 0, EPI_V = 1, EPI_Q = 2, EPI_G = 3 };
constexpr int PF = 4;

template <int RB> struct AsState {
  bf16x8 a[RB][32];
  bf16x8 bw[PF];
  f32x16 acc[2][RB];
};

DI bf16x8 wfrag(lds_cptr base_c, int s) { return cat8(trrd(base_c + s * 2048), trrd(base_c + s * 2048 + 512)); }

template <int EPI, int RB> DI void as_store(const f32x16 (&acc)[RB], char* outp  , int part) {
  const int rb = part >> 1, sp = part & 1;
  if constexpr (EPI == EPI_G) {
    if (sp == 0) {
      float g[8];
#pragma unroll
      for (int j = 0; j < 8; ++j) {
        const float x = acc[rb][j];
        const float z = x * (1.f + 0.044715f * x * x) * (-2.f * 0.7978845608028654f * 1.4426950408889634f);
        g[j] = x * __builtin_amdgcn_rcpf(1.f + __builtin_amdgcn_exp2f(z)) * acc[rb][8 + j];
      }
      u32x4 o = {cvtpk(g[0], g[1]), cvtpk(g[2], g[3]), cvtpk(g[4], g[5]), cvtpk(g[6], g[7])};
      *(u32x4*)(outp + (size_t)rb * (FF / 16) * 1024) = o;
    }
  } else {
    const float sc = EPI == EPI_Q ? QSCALE : 1.f;
    u32x4 o;
#pragma unroll
    for (int j = 0; j < 4; ++j) o[j] = EPI == EPI_Q ? cvtpk(acc[rb][8 * sp + 2 * j] * sc, acc[rb][8 * sp + 2 * j + 1] * sc) : cvtpk(acc[rb][8 * sp + 2 * j], acc[rb][8 * sp + 2 * j + 1]);
    *(u32x4*)(outp + rb * 8192 + sp * (EPI == EPI_V ? 4096 : 1024)) = o;
  }
}

template <int EPI, int PEPI, int RB, int C, bool LAST_OF_TILE, bool HAS_NEXT, int NVM, class DmaF>
DI void as_half(AsState<RB>& st, lds_cptr cur, lds_cptr nxt, char* pout, DmaF&& dma) {
#pragma unroll
  for (int r = 0; r < RB; ++r) st.acc[C][r] = f32x16{};
#pragma unroll
  for (int s = 0; s < 32; ++s) {
    const bf16x8 b = st.bw[s % PF];
    if (LAST_OF_TILE && HAS_NEXT && s == 32 - PF) {
      asm volatile("s_waitcnt vmcnt(%0) lgkmcnt(0)\n\ts_barrier" ::"i"(NVM) : "memory");
    }
    if constexpr (EPI == EPI_V) st.acc[C][0] = MFMA(st.a[0][s], b, st.acc[C][0]); else st.acc[C][0] = MFMA(b, st.a[0][s], st.acc[C][0]);
    if (s + PF < 32) st.bw[s % PF] = wfrag(cur, s + PF);
    else if (HAS_NEXT) st.bw[s % PF] = wfrag(nxt, s + PF - 32);
    if constexpr (RB == 2) {
      if constexpr (EPI == EPI_V) st.acc[C][1] = MFMA(st.a[1][s], b, st.acc[C][1]); else st.acc[C][1] = MFMA(b, st.a[1][s], st.acc[C][1]);
    }
    if (C == 0 && s < 16) dma(s);
    if constexpr (PEPI >= 0) { if (s >= 16 && s < 16 + 4 * RB && ((s - 16) & 1) == 0) as_store<PEPI, RB>(st.acc[C ^ 1], pout, (s - 16) >> 1); }
    SBAR();
  }
}

struct AsLane { unsigned voff; unsigned rd0, rd1; };

template <int MODE>
__global__ __launch_bounds__(256, 1) void k_proj(const u16* __restrict__ A, const u16* __restrict__ W0, const u16* __restrict__ W1, char* __restrict__ out0, char* __restrict__ out1) {
  extern __shared__ __attribute__((aligned(16))) char lds[];
  const int tid = threadIdx.x, lane = tid & 63, w = rfl(tid >> 6), l31 = lane & 31, h = lane >> 5;
  const int b = blockIdx.x, head = (b & 7) * 4 + ((b >> 3) & 3), rg = b >> 5;
  constexpr int NT = MODE == 0 ? 4 : 2;
  const __amdgpu_buffer_rsrc_t s0 = mk_srd(W0, 512u * 4096u * 2u), s1 = mk_srd(MODE == 0 ? W1 : W0, 512u * 4096u * 2u);
  const unsigned voff = (lane >> 3) * 8192u + (((lane & 7) ^ (6 * ((lane >> 4) & 1))) * 16u);
  const unsigned ldsb = (unsigned)(size_t)lds;
  auto dma_tile = [&](int t, int p) {
    const unsigned so = (unsigned)(w * 128 + p * 8) * 8192u + (unsigned)(head * 256 + (t & 1) * 128);
    dma16(ldsb + (t & 1) * 65536u + (unsigned)(w * 16 + p) * 1024u, voff, (t < 2) ? s0 : s1, so);
  };
#pragma unroll
  for (int p = 0; p < 16; ++p) dma_tile(0, p);
  AsState<2> st;
#pragma unroll
  for (int s = 0; s < 32; ++s)
#pragma unroll
    for (int r = 0; r < 2; ++r) {
      if (MODE == 0) st.a[r][s] = *(const bf16x8*)(A + (size_t)(rg * 256 + w * 64 + r * 32 + l31) * DM + s * 16 + h * 8);
      else st.a[r][s] = *(const bf16x8*)(A + ((size_t)((rg * 8 + w * 2 + r) * 32 + s) * 64 + lane) * 8);
    }
  const int q = (lane & 15) >> 2, p4 = lane & 3, blk = (lane >> 4) & 1;
  const unsigned sw = ((q >> 1) & 1) * 96u;
  const unsigned r0 = (8 * h + q) * 128u + ((0 * 64u + blk * 32u + p4 * 8u) ^ sw), r1 = (8 * h + q) * 128u + ((64u + blk * 32u + p4 * 8u) ^ sw);
  const lds_cptr L = (lds_cptr)lds;
  asm volatile("s_waitcnt vmcnt(0)\n\ts_barrier" ::: "memory");
#pragma unroll
  for (int s = 0; s < PF; ++s) st.bw[s] = wfrag(L + r0, s);
  char* const oK = out0 + ((size_t)(head * 64 + rg * 8 + w * 2) * 8) * 1024 + lane * 16;
  char* const oV = out1 + ((size_t)(head * 128 + (rg * 8 + w * 2) * 2) * 4) * 1024 + lane * 16;
  constexpr int E0 = MODE == 0 ? EPI_K : EPI_Q;
  auto nod = [&](int) {};
  as_half<E0, -1, 2, 0, false, true, 0>(st, L + r0, L + r1, nullptr, [&](int p) { dma_tile(1, p); });
  as_half<E0, E0, 2, 1, true, true, 4>(st, L + r1, L + 65536 + r0, oK, nod);
  if constexpr (MODE == 0) {
    as_half<E0, E0, 2, 0, false, true, 0>(st, L + 65536 + r0, L + 65536 + r1, oK + 2048, [&](int p) { dma_tile(2, p); });
    as_half<E0, E0, 2, 1, true, true, 4>(st, L + 65536 + r1, L + r0, oK + 4096, nod);
    as_half<EPI_V, E0, 2, 0, false, true, 0>(st, L + r0, L + r1, oK + 6144, [&](int p) { dma_tile(3, p); });
    as_half<EPI_V, EPI_V, 2, 1, true, true, 4>(st, L + r1, L + 65536 + r0, oV, nod);
    as_half<EPI_V, EPI_V, 2, 0, false, true, 0>(st, L + 65536 + r0, L + 65536 + r1, oV + 1024, nod);
    as_half<EPI_V, EPI_V, 2, 1, true, false, 0>(st, L + 65536 + r1, L, oV + 2048, nod);
#pragma unroll
    for (int p = 0; p < 4; ++p) as_store<EPI_V, 2>(st.acc[1], oV + 3072, p);
  } else {
    as_half<E0, E0, 2, 0, false, true, 0>(st, L + 65536 + r0, L + 65536 + r1, oK + 2048, nod);
    as_half<E0, E0, 2, 1, true, false, 0>(st, L + 65536 + r1, L, oK + 4096, nod);
#pragma unroll
    for (int p = 0; p < 4; ++p) as_store<E0, 2>(st.acc[1], oK + 6144, p);
  }
}

__global__ __launch_bounds__(256, 1) void k_gateup(const u16* __restrict__ A, const u16* __restrict__ Wg, const u16* __restrict__ Wu, char* __restrict__ G) {
  extern __shared__ __attribute__((aligned(16))) char lds[];
  const int tid = threadIdx.x, lane = tid & 63, w = rfl(tid >> 6), h = lane >> 5;
  const int b = blockIdx.x;
  const int cg = b % 14, rg = b / 14;
  const unsigned ch = (lane & 7) ^ (6 * ((lane >> 4) & 1));
  const long dlt = (const char*)Wu - (const char*)Wg;
  const char* lo = dlt >= 0 ? (const char*)Wg : (const char*)Wu;
  (void)lo;
  const unsigned ldsb = (unsigned)(size_t)lds;
  const char* src = (ch < 4 ? (const char*)Wg : (const char*)Wu) + (size_t)(w * 128 + (lane >> 3)) * (FF * 2) + cg * 256 + (ch & 3) * 16;
  auto dma_tile = [&](int t, int p) {
    const char* g = src + (size_t)p * 8 * (FF * 2) + t * 64;
    const unsigned m0v = ldsb + (t & 1) * 65536u + (unsigned)(w * 16 + p) * 1024u;
    asm volatile("s_mov_b32 m0, %0\n\ts_nop 0\n\tglobal_load_lds_dwordx4 %1, off" ::"s"(m0v), "v"(g) : "memory");
  };
#pragma unroll
  for (int p = 0; p < 16; ++p) dma_tile(0, p);
  AsState<1> st;
#pragma unroll
  for (int s = 0; s < 32; ++s) st.a[0][s] = *(const bf16x8*)(A + ((size_t)((rg * 4 + w) * 32 + s) * 64 + lane) * 8);
  const int q = (lane & 15) >> 2, p4 = lane & 3, blk = (lane >> 4) & 1;
  const unsigned sw = ((q >> 1) & 1) * 96u;
  const unsigned r0 = (8 * h + q) * 128u + ((blk * 64u + 0u + p4 * 8u) ^ sw), r1 = (8 * h + q) * 128u + ((blk * 64u + 32u + p4 * 8u) ^ sw);
  const lds_cptr L = (lds_cptr)lds;
  asm volatile("s_waitcnt vmcnt(0)\n\ts_barrier" ::: "memory");
#pragma unroll
  for (int s = 0; s < PF; ++s) st.bw[s] = wfrag(L + r0, s);
  char* const o = G + ((size_t)(rg * 4 + w) * (FF / 16) + cg * 8) * 1024 + lane * 16;
  auto nod = [&](int) {};
  as_half<EPI_G, -1, 1, 0, false, true, 0>(st, L + r0, L + r1, nullptr, [&](int p) { dma_tile(1, p); });
  as_half<EPI_G, EPI_G, 1, 1, true, true, 1>(st, L + r1, L + 65536 + r0, o, nod);
  as_half<EPI_G, EPI_G, 1, 0, false, true, 0>(st, L + 65536 + r0, L + 65536 + r1, o + 1024, [&](int p) { dma_tile(2, p); });
  as_half<EPI_G, EPI_G, 1, 1, true, true, 1>(st, L + 65536 + r1, L + r0, o + 2048, nod);
  as_half<EPI_G, EPI_G, 1, 0, false, true, 0>(st, L + r0, L + r1, o + 3072, [&](int p) { dma_tile(3, p); });
  as_half<EPI_G, EPI_G, 1, 1, true, true, 1>(st, L + r1, L + 65536 + r0, o + 4096, nod);
  as_half<EPI_G, EPI_G, 1, 0, false, true, 0>(st, L + 65536 + r0, L + 65536 + r1, o + 5120, nod);
  as_half<EPI_G, EPI_G, 1, 1, true, false, 0>(st, L + 65536 + r1, L, o + 6144, nod);
  as_store<EPI_G, 1>(st.acc[1], o + 7168, 0);
}

template <int KS  , int EPI>
__global__ __launch_bounds__(256, 1) void k_nk(const char* __restrict__ AF, const u16* __restrict__ W, const u16* __restrict__ res, const float* __restrict__ gvec, u16* __restrict__ out) {
  extern __shared__ __attribute__((aligned(16))) char lds[];
  constexpr int NS = 8, D = NS - 1, KT = KS * 4;
  const int tid = threadIdx.x, lane = tid & 63, w = rfl(tid >> 6), l31 = lane & 31, h = lane >> 5;
  const int b = blockIdx.x, x = b & 7, i = b >> 3;
  const int rb = x * 4 + (i & 3), nb = i >> 2;
  const __amdgpu_buffer_rsrc_t sw_ = mk_srd(W, (unsigned)KT * 16u * 1024u), sa = mk_srd(AF, (unsigned)SEQ * KT * 32u);
  const unsigned wv = (lane >> 3) * 1024u + (((lane & 7) ^ (6 * ((lane >> 4) & 1))) * 16u) + nb * 128u;
  const unsigned ring = (unsigned)(size_t)lds + w * (NS * 2048u);
  const unsigned av0 = lane * 16u + (unsigned)(rb * 2) * KT * 1024u, av1 = av0 + KT * 1024u;
  bf16x8 a[NS][2];
  auto issue = [&](int s) {
    const unsigned ks = (unsigned)(w * KS + s);
    dma16(ring + (s % NS) * 2048u, wv, sw_, ks * 16384u);
    dma16(ring + (s % NS) * 2048u + 1024u, wv, sw_, ks * 16384u + 8192u);
    asm volatile("buffer_load_dwordx4 %0, %1, %2, %3 offen" : "=v"(a[s % NS][0]) : "v"(av0), "s"(sa), "s"(ks * 1024u) : "memory");
    asm volatile("buffer_load_dwordx4 %0, %1, %2, %3 offen" : "=v"(a[s % NS][1]) : "v"(av1), "s"(sa), "s"(ks * 1024u) : "memory");
  };
#pragma unroll
  for (int s = 0; s < D; ++s) issue(s);
  const int q = (lane & 15) >> 2, p4 = lane & 3, blk = (lane >> 4) & 1, pp = ((p4 & 1) << 1) | (p4 >> 1);
  const unsigned swz = ((q >> 1) & 1) * 96u;
  const lds_cptr R = (lds_cptr)lds + w * (NS * 2048);
  const unsigned r0 = (4 * h + q) * 128u + ((blk * 32u + pp * 8u) ^ swz), r1 = (4 * h + q) * 128u + ((64u + blk * 32u + pp * 8u) ^ swz);
  f32x16 acc[2][2] = {};
#pragma unroll
  for (int s = 0; s < KS; ++s) {
    constexpr int dummy = 0; (void)dummy;
    const int rem = (KS - 1 - s) < (D - 1) ? (KS - 1 - s) : (D - 1);
    switch (rem) {
      case 0: asm volatile("s_waitcnt vmcnt(0)" : "+v"(a[s % NS][0]), "+v"(a[s % NS][1])::"memory"); break;
      case 1: asm volatile("s_waitcnt vmcnt(4)" : "+v"(a[s % NS][0]), "+v"(a[s % NS][1])::"memory"); break;
      case 2: asm volatile("s_waitcnt vmcnt(8)" : "+v"(a[s % NS][0]), "+v"(a[s % NS][1])::"memory"); break;
      case 3: asm volatile("s_waitcnt vmcnt(12)" : "+v"(a[s % NS][0]), "+v"(a[s % NS][1])::"memory"); break;
      case 4: asm volatile("s_waitcnt vmcnt(16)" : "+v"(a[s % NS][0]), "+v"(a[s % NS][1])::"memory"); break;
      case 5: asm volatile("s_waitcnt vmcnt(20)" : "+v"(a[s % NS][0]), "+v"(a[s % NS][1])::"memory"); break;
      default: asm volatile("s_waitcnt vmcnt(24)" : "+v"(a[s % NS][0]), "+v"(a[s % NS][1])::"memory"); break;
    }
    const lds_cptr sl = R + (s % NS) * 2048;
    const bf16x8 b0 = cat8(trrd(sl + r0), trrd(sl + r0 + 1024)), b1 = cat8(trrd(sl + r1), trrd(sl + r1 + 1024));
    if (s + D < KS) issue(s + D);
    acc[0][0] = MFMA(b0, a[s % NS][0], acc[0][0]);
    acc[1][0] = MFMA(b0, a[s % NS][1], acc[1][0]);
    acc[0][1] = MFMA(b1, a[s % NS][0], acc[0][1]);
    acc[1][1] = MFMA(b1, a[s % NS][1], acc[1][1]);
    SBAR();
  }
  __syncthreads();
  f32x4* X = (f32x4*)lds;
#pragma unroll
  for (int d = 0; d < 4; ++d) {
    if (d != w) {
      const int slot = (w - d - 1) & 3;
#pragma unroll
      for (int g = 0; g < 4; ++g) {
        const f32x16& v = acc[d >> 1][d & 1];
        X[((d * 3 + slot) * 4 + g) * 64 + lane] = (f32x4){v[4 * g], v[4 * g + 1], v[4 * g + 2], v[4 * g + 3]};
      }
    }
  }
  __syncthreads();
  f32x16 mine = w == 0 ? acc[0][0] : w == 1 ? acc[0][1] : w == 2 ? acc[1][0] : acc[1][1];
#pragma unroll
  for (int sl = 0; sl < 3; ++sl)
#pragma unroll
    for (int g = 0; g < 4; ++g) {
      const f32x4 v = X[((w * 3 + sl) * 4 + g) * 64 + lane];
#pragma unroll
      for (int e = 0; e < 4; ++e) mine[4 * g + e] += v[e];
    }
  const int rblk = rb * 2 + (w >> 1), row = rblk * 32 + l31, n0 = nb * 64 + (w & 1) * 32;
#pragma unroll
  for (int sp = 0; sp < 2; ++sp) {
    const int n = n0 + 16 * sp + 8 * h;
    const size_t foff = ((size_t)(rblk * 32 + (n >> 4)) * 64 + lane) * 8, roff = (size_t)row * DM + n;
    const u32x4 xr = *(const u32x4*)(res + (EPI == 0 ? roff : foff));
    const f32x4 g0 = *(const f32x4*)(gvec + n), g1 = *(const f32x4*)(gvec + n + 4);
    u32x4 o;
#pragma unroll
    for (int j = 0; j < 2; ++j) {
      o[j] = cvtpk(bf_lo(xr[j]) + g0[2 * j] * mine[8 * sp + 2 * j], bf_hi(xr[j]) + g0[2 * j + 1] * mine[8 * sp + 2 * j + 1]);
      o[2 + j] = cvtpk(bf_lo(xr[2 + j]) + g1[2 * j] * mine[8 * sp + 4 + 2 * j], bf_hi(xr[2 + j]) + g1[2 * j + 1] * mine[8 * sp + 4 + 2 * j + 1]);
    }
    *(u32x4*)(out + (EPI == 0 ? foff : roff)) = o;
  }
}

#define MX3(a, b, c) __builtin_fmaxf(__builtin_fmaxf((a), (b)), (c))
__global__ __launch_bounds__(512, 2) void k_attn(const char* __restrict__ QF, const char* __restrict__ KF, const char* __restrict__ VF, char* __restrict__ CF) {
  extern __shared__ __attribute__((aligned(16))) char lds[];
  const int tid = threadIdx.x, lane = tid & 63, w = rfl(tid >> 6), l31 = lane & 31, h = lane >> 5, g = w >> 2, qq = w & 3;
  const int b = blockIdx.x, head = (b & 7) * 4 + ((b >> 3) & 3), j = b >> 5;
  const __amdgpu_buffer_rsrc_t sk = mk_srd(KF, (unsigned)SEQ * NH * HD * 2u), sv = mk_srd(VF, (unsigned)SEQ * NH * HD * 2u);
  const unsigned ldsg = (unsigned)(size_t)lds + g * 65536u + qq * 4096u;
  const unsigned voff = lane * 16u + qq * 4096u;
  const unsigned hb = (unsigned)head * 32u * 16384u + g * 16384u;
  auto dmaK = [&](int t) {
#pragma unroll
    for (int p = 0; p < 4; ++p) dma16(ldsg + (t & 1) * 16384u + p * 1024u, voff, sk, hb + (unsigned)t * 32768u + p * 1024u);
  };
  auto dmaV = [&](int t) {
#pragma unroll
    for (int p = 0; p < 4; ++p) dma16(ldsg + 32768u + (t & 1) * 16384u + p * 1024u, voff, sv, hb + (unsigned)t * 32768u + p * 1024u);
  };
  const lds_cptr LG = (lds_cptr)lds + g * 65536 + lane * 16;
  for (int pass = 0; pass < 2; ++pass) {
    const int qb = pass == 0 ? 15 - j : j, T = qb + 1;
    dmaK(0); dmaV(0); dmaK(1);
    bf16x8 qf[8];
#pragma unroll
    for (int s = 0; s < 8; ++s) qf[s] = *(const bf16x8*)(QF + ((size_t)(head * 64 + qb * 4 + qq) * 8 + s) * 1024 + lane * 16);
    f32x16 O[4] = {}, negm = {};
    float m = 0.f, l = 0.f;
    u32x4 P[4];
    asm volatile("s_waitcnt vmcnt(0)\n\ts_barrier" ::: "memory");
    if (g == 1) asm volatile("s_barrier" ::: "memory");
    for (int t = 0; t < T; ++t) {
      f32x16 S0, S1;
      {
        const lds_cptr kp = LG + (t & 1) * 16384;
        __builtin_amdgcn_s_setprio(1);
#pragma unroll
        for (int s = 0; s < 8; ++s) {
          const bf16x8 k0 = *(const LAS bf16x8*)(kp + s * 1024), k1 = *(const LAS bf16x8*)(kp + 8192 + s * 1024);
          S0 = MFMA(k0, qf[s], s == 0 ? negm : S0);
          S1 = MFMA(k1, qf[s], s == 0 ? negm : S1);
        }
        if (t > 0) {
          const lds_cptr vp = LG + 32768 + ((t - 1) & 1) * 16384;
#pragma unroll
          for (int kt = 0; kt < 4; ++kt)
#pragma unroll
            for (int db = 0; db < 4; ++db) {
              const bf16x8 v = *(const LAS bf16x8*)(vp + (kt * 4 + db) * 1024);
              O[db] = MFMA(v, __builtin_bit_cast(bf16x8, P[kt]), O[db]);
            }
        }
        __builtin_amdgcn_s_setprio(0);
      }
      asm volatile("s_waitcnt lgkmcnt(0)\n\ts_barrier" ::: "memory");
      dmaK(t + 2); dmaV(t + 1);
      if (t == T - 1) {
        const int rel = 32 * qq + l31 - 64 * g - 4 * h;
#pragma unroll
        for (int r = 0; r < 16; ++r) {
          const int c = (r & 3) + 8 * (r >> 2);
          if (c > rel) S0[r] = -INFINITY;
          if (c + 32 > rel) S1[r] = -INFINITY;
        }
      }
      float ra = MX3(S0[0], S0[1], S1[0]), rb = MX3(S0[2], S0[3], S1[1]); ra = MX3(ra, S1[2], S1[3]);
#pragma unroll
      for (int r = 4; r < 16; r += 4) { ra = MX3(ra, S0[r], S0[r + 1]); rb = MX3(rb, S0[r + 2], S0[r + 3]); ra = MX3(ra, S1[r], S1[r + 1]); rb = MX3(rb, S1[r + 2], S1[r + 3]); }
      float rm = __builtin_fmaxf(ra, rb);
      { auto rr = __builtin_amdgcn_permlane32_swap(__float_as_uint(rm), __float_as_uint(rm), false, false);
        rm = __builtin_fmaxf(__uint_as_float(rr[0]), __uint_as_float(rr[1])); }
      if (t == 0 || __any(rm > THR)) {
        const float dl = __builtin_fmaxf(rm, t == 0 ? -1e30f : 0.f);
        const float f = t == 0 ? 0.f : __builtin_amdgcn_exp2f(-dl);
        m += dl; l *= f;
#pragma unroll
        for (int r = 0; r < 16; ++r) { S0[r] -= dl; S1[r] -= dl; negm[r] = -m; }
#pragma unroll
        for (int d = 0; d < 4; ++d)
#pragma unroll
          for (int r = 0; r < 16; ++r) O[d][r] *= f;
      }
      float s0 = 0.f, s1 = 0.f;
#pragma unroll
      for (int r = 0; r < 16; ++r) { S0[r] = __builtin_amdgcn_exp2f(S0[r]); S1[r] = __builtin_amdgcn_exp2f(S1[r]); s0 += S0[r]; s1 += S1[r]; }
      l += s0 + s1;
#pragma unroll
      for (int e = 0; e < 4; ++e) {
        P[0][e] = cvtpk(S0[2 * e], S0[2 * e + 1]); P[1][e] = cvtpk(S0[8 + 2 * e], S0[8 + 2 * e + 1]);
        P[2][e] = cvtpk(S1[2 * e], S1[2 * e + 1]); P[3][e] = cvtpk(S1[8 + 2 * e], S1[8 + 2 * e + 1]);
      }
      asm volatile("s_waitcnt vmcnt(8)\n\ts_barrier" ::: "memory");
    }
    {
      const lds_cptr vp = LG + 32768 + ((T - 1) & 1) * 16384;
#pragma unroll
      for (int kt = 0; kt < 4; ++kt)
#pragma unroll
        for (int db = 0; db < 4; ++db) {
          const bf16x8 v = *(const LAS bf16x8*)(vp + (kt * 4 + db) * 1024);
          O[db] = MFMA(v, __builtin_bit_cast(bf16x8, P[kt]), O[db]);
        }
    }
    if (g == 0) asm volatile("s_barrier" ::: "memory");
    asm volatile("s_waitcnt vmcnt(0) lgkmcnt(0)\n\ts_barrier" ::: "memory");
    { auto rr = __builtin_amdgcn_permlane32_swap(__float_as_uint(l), __float_as_uint(l), false, false); l = __uint_as_float(rr[0]) + __uint_as_float(rr[1]); }
    f32x4* X = (f32x4*)(lds + w * 8192);
    float* ML = (float*)(lds + 65536) + w * 128;
    ML[lane] = m; ML[64 + lane] = l;
    if (g == 0) {
#pragma unroll
      for (int d = 0; d < 2; ++d)
#pragma unroll
        for (int e = 0; e < 4; ++e) X[(d * 4 + e) * 64 + lane] = (f32x4){O[2 + d][4 * e], O[2 + d][4 * e + 1], O[2 + d][4 * e + 2], O[2 + d][4 * e + 3]};
    } else {
#pragma unroll
      for (int d = 0; d < 2; ++d)
#pragma unroll
        for (int e = 0; e < 4; ++e) X[(d * 4 + e) * 64 + lane] = (f32x4){O[d][4 * e], O[d][4 * e + 1], O[d][4 * e + 2], O[d][4 * e + 3]};
      O[0] = O[2]; O[1] = O[3];
    }
    __syncthreads();
    {
      const f32x4* Y = (const f32x4*)(lds + (w ^ 4) * 8192);
      const float* ML2 = (const float*)(lds + 65536) + (w ^ 4) * 128;
      const float m2 = ML2[lane], l2 = ML2[64 + lane];
      const float mn = __builtin_fmaxf(m, m2);
      float fa = __builtin_amdgcn_exp2f(m - mn), fb = __builtin_amdgcn_exp2f(m2 - mn);
      const float inv = __builtin_amdgcn_rcpf(l * fa + l2 * fb);
      fa *= inv; fb *= inv;
      char* op = CF + ((size_t)(qb * 4 + qq) * 256 + head * 8 + g * 4) * 1024 + lane * 16;
#pragma unroll
      for (int d = 0; d < 2; ++d)
#pragma unroll
        for (int sp = 0; sp < 2; ++sp) {
          const f32x4 y0 = Y[(d * 4 + 2 * sp) * 64 + lane], y1 = Y[(d * 4 + 2 * sp + 1) * 64 + lane];
          float o[8];
#pragma unroll
          for (int e = 0; e < 4; ++e) { o[e] = O[d][8 * sp + e] * fa + y0[e] * fb; o[4 + e] = O[d][8 * sp + 4 + e] * fa + y1[e] * fb; }
          u32x4 ov = {cvtpk(o[0], o[1]), cvtpk(o[2], o[3]), cvtpk(o[4], o[5]), cvtpk(o[6], o[7])};
          *(u32x4*)(op + (d * 2 + sp) * 1024) = ov;
        }
    }
    __syncthreads();
  }
}

extern "C" void kernel_launch(void* const* d_in, const int* in_sizes, int n_in, void* d_out, int out_size, void* d_ws, size_t ws_size, hipStream_t stream) {
  (void)in_sizes; (void)n_in; (void)out_size; (void)ws_size;
  const u16* x = (const u16*)d_in[0]; const u16* ckv = (const u16*)d_in[1];
  const u16* wq = (const u16*)d_in[2]; const u16* wuk = (const u16*)d_in[3]; const u16* wuv = (const u16*)d_in[4]; const u16* wo = (const u16*)d_in[5];
  const u16* wg = (const u16*)d_in[7]; const u16* wu = (const u16*)d_in[8]; const u16* wd = (const u16*)d_in[9];
  const u16* c = (const u16*)d_in[10]; const u16* wmod = (const u16*)d_in[11];
  char* ws = (char*)d_ws;
  float* mod = (float*)(ws + WS_MOD);
  static bool attr = false;
  if (!attr) {
    attr = true;
    (void)hipFuncSetAttribute((const void*)k_proj<0>, hipFuncAttributeMaxDynamicSharedMemorySize, 131072);
    (void)hipFuncSetAttribute((const void*)k_proj<1>, hipFuncAttributeMaxDynamicSharedMemorySize, 131072);
    (void)hipFuncSetAttribute((const void*)k_gateup, hipFuncAttributeMaxDynamicSharedMemorySize, 131072);
    (void)hipFuncSetAttribute((const void*)k_attn, hipFuncAttributeMaxDynamicSharedMemorySize, 131072);
    (void)hipFuncSetAttribute((const void*)k_nk<64, 0>, hipFuncAttributeMaxDynamicSharedMemorySize, 65536);
    (void)hipFuncSetAttribute((const void*)k_nk<28, 1>, hipFuncAttributeMaxDynamicSharedMemorySize, 65536);
  }
  k_mod<<<96, 256, 0, stream>>>(c, wmod, mod);
  k_ln<false><<<512, 256, 0, stream>>>(x, mod, mod + 512, (u16*)(ws + WS_HF));
  k_proj<0><<<256, 256, 131072, stream>>>(ckv, wuk, wuv, ws + WS_KF, ws + WS_VF);
  k_proj<1><<<256, 256, 131072, stream>>>((const u16*)(ws + WS_HF), wq, wq, ws + WS_QF, ws + WS_QF);
  k_attn<<<256, 512, 131072, stream>>>(ws + WS_QF, ws + WS_KF, ws + WS_VF, ws + WS_CF);
  k_nk<64, 0><<<256, 256, 65536, stream>>>(ws + WS_CF, wo, x, mod + 1024, (u16*)(ws + WS_X1F));
  k_ln<true><<<512, 256, 0, stream>>>((const u16*)(ws + WS_X1F), mod + 1536, mod + 2048, (u16*)(ws + WS_H2F));
  k_gateup<<<224, 256, 131072, stream>>>((const u16*)(ws + WS_H2F), wg, wu, ws + WS_GF);
  k_nk<28, 1><<<256, 256, 65536, stream>>>(ws + WS_GF, wd, (const u16*)(ws + WS_X1F), mod + 2560, (u16*)d_out);
}
```

```cpp
#include <hip/hip_runtime.h>
#include <hip/hip_bf16.h>
#include <cstdint>

typedef unsigned short u16;
typedef short bf16x8 __attribute__((ext_vector_type(8)));
typedef short s16x4 __attribute__((ext_vector_type(4)));
typedef short v4i16_t __attribute__((ext_vector_type(4)));
typedef float f32x16 __attribute__((ext_vector_type(16)));
typedef float f32x4 __attribute__((ext_vector_type(4)));
typedef float f32x2 __attribute__((ext_vector_type(2)));
typedef __bf16 bf16x2_t __attribute__((ext_vector_type(2)));
typedef unsigned u32x4 __attribute__((ext_vector_type(4)));
typedef unsigned u32x2 __attribute__((ext_vector_type(2)));
#define LAS __attribute__((address_space(3)))
typedef LAS const char* lds_cptr;
typedef LAS char* lds_ptr;
#define DI __device__ __forceinline__
#define SBAR() __builtin_amdgcn_sched_barrier(0)
#define MFMA(a, b, c) __builtin_amdgcn_mfma_f32_32x32x16_bf16((a), (b), (c), 0, 0, 0)
__device__ __forceinline__ void mfma16(f32x4& c, const bf16x8& a, const bf16x8& b, bool first) {
  if (first) asm volatile("v_mfma_f32_16x16x32_bf16 %0, %1, %2, 0" : "=&v"(c) : "v"(a), "v"(b));
  else asm volatile("v_mfma_f32_16x16x32_bf16 %0, %1, %2, %0" : "+v"(c) : "v"(a), "v"(b));
}

constexpr int SEQ = 2048, DM = 512, NH = 32, HD = 128, FF = 1792;
constexpr float QSCALE = 0.08838834764831845f * 1.4426950408889634f;
constexpr float THR = 8.0f;

constexpr size_t WS_MOD = 0;
constexpr size_t WS_GR  = 16384;
constexpr size_t WS_EP  = 16384 + 4096 + 256;
constexpr size_t WS_F1  = 32768;
constexpr size_t WS_F2  = WS_F1 + 8192;
constexpr size_t WS_F3  = WS_F2 + 4096;
constexpr size_t WS_F0  = WS_F3 + 4096;
constexpr size_t WS_HF  = 65536;
constexpr size_t WS_X1F = WS_HF + (size_t)SEQ * DM * 2;
constexpr size_t WS_H2F = WS_X1F + (size_t)SEQ * DM * 2;
constexpr size_t WS_KF  = WS_H2F + (size_t)SEQ * DM * 2;
constexpr unsigned KFL  = (unsigned)SEQ * NH * HD * 2u;
constexpr size_t WS_HFL = WS_KF + KFL + 4096;
constexpr size_t WS_VF  = WS_KF + KFL + 65536;
constexpr size_t WS_CF  = WS_VF + (size_t)SEQ * NH * HD * 2;
constexpr size_t WS_GF  = WS_CF + (size_t)SEQ * NH * HD * 2;
constexpr size_t WS_PP  = WS_GF + (size_t)SEQ * FF * 2;
constexpr size_t WS_END = WS_PP + (size_t)4 * SEQ * DM * 2;

DI unsigned cvtpk(float lo, float hi) { f32x2 v = {lo, hi}; return __builtin_bit_cast(unsigned, __builtin_convertvector(v, bf16x2_t)); }
DI float bf_lo(unsigned w) { return __uint_as_float(w << 16); }
DI float bf_hi(unsigned w) { return __uint_as_float(w & 0xffff0000u); }
DI float rbf(float x) { return bf_lo(cvtpk(x, x)); }
DI s16x4 trrd(lds_cptr p) { return __builtin_bit_cast(s16x4, __builtin_amdgcn_ds_read_tr16_b64_v4i16((LAS v4i16_t*)p)); }
DI bf16x8 cat8(s16x4 a, s16x4 b) { return __builtin_shufflevector(a, b, 0, 1, 2, 3, 4, 5, 6, 7); }
DI bf16x8 rd128(lds_cptr p) { return *(const LAS bf16x8*)p; }
DI unsigned rfl(unsigned x) { return __builtin_amdgcn_readfirstlane(x); }
template <int NOP = 0> DI void dma16(unsigned m0v, unsigned voff, __amdgpu_buffer_rsrc_t srd, unsigned soff) {
  asm volatile("s_mov_b32 m0, %0\n\ts_nop %4\n\tbuffer_load_dwordx4 %1, %2, %3 offen lds" ::"s"(m0v), "v"(voff), "s"(srd), "s"(soff), "i"(NOP) : "memory");
}
DI __amdgpu_buffer_rsrc_t mk_srd(const void* p, unsigned bytes) { return __builtin_amdgcn_make_buffer_rsrc((void*)p, (short)0, (int)bytes, 0x00020000); }
#define NOSIDE 0
#define PRO 3
#define ROT 0
#define NTA 0
#define NTW 0
#define NTS ""
#define WSEND 1
#define AUXKV 16
#define AUXPP 16
#define AUXG 16
#define AUXC 16
#define AUXH 16
#define AUXO 16
#define AUXR 16
template <int AUX> DI void st16(void* p, u32x4 v) {
  if constexpr (AUX == 0) *(u32x4*)p = v;
  else if constexpr (AUX == 16) asm volatile("global_store_dwordx4 %0, %1, off sc1\n\ts_nop 1" ::"v"(p), "v"(v) : "memory");
  else if constexpr (AUX == 2) asm volatile("global_store_dwordx4 %0, %1, off nt\n\ts_nop 1" ::"v"(p), "v"(v) : "memory");
  else if constexpr (AUX == 17) asm volatile("global_store_dwordx4 %0, %1, off sc0 sc1\n\ts_nop 1" ::"v"(p), "v"(v) : "memory");
  else asm volatile("global_store_dwordx4 %0, %1, off sc1 nt\n\ts_nop 1" ::"v"(p), "v"(v) : "memory");
}
#define WAITBAR(str) asm volatile("s_waitcnt " str "\n\ts_barrier" ::: "memory")

#define STAMP(i)
#define RSTAMP(i)
#define STAMP_DECL
#define STAMP_OUT(k, nw)
#define PROF_ARG
#define PROF_PASS
#define PROF_REF
#define PROF_FWD

template <int CTRL, int RM> DI float dppadd(float v) { return v + __int_as_float(__builtin_amdgcn_update_dpp(0, __float_as_int(v), CTRL, RM, 0xf, false)); }
DI float wsum63(float v) {
  v = dppadd<0xB1, 0xf>(v); v = dppadd<0x4E, 0xf>(v); v = dppadd<0x141, 0xf>(v); v = dppadd<0x140, 0xf>(v);
  v = dppadd<0x142, 0xa>(v); v = dppadd<0x143, 0xc>(v);
  return v;
}
DI float wsum(float v) { return __int_as_float(__builtin_amdgcn_readlane(__float_as_int(wsum63(v)), 63)); }

struct GuP {
  lds_cptr L; unsigned a0, b0, b1;
  unsigned ldsb, vA, vG, vU, sA, sW;
  __amdgpu_buffer_rsrc_t rA, rG, rU;
  int w;
};
DI void gu_dma(const GuP& p, int piece, unsigned slot, int st) {
  if (piece < 2) dma16(p.ldsb + slot + p.w * 2048u + piece * 1024u, p.vA, p.rA, p.sA + st * 4096u + piece * 1024u);
  else if (piece < 4) dma16(p.ldsb + slot + 16384u + p.w * 2048u + (piece - 2) * 1024u, p.vG, p.rG, p.sW + st * (64u * FF * 2u) + (piece - 2) * (4u * FF * 2u));
  else dma16(p.ldsb + slot + 32768u + p.w * 2048u + (piece - 4) * 1024u, p.vU, p.rU, p.sW + st * (64u * FF * 2u) + (piece - 4) * (4u * FF * 2u));
}
DI void gu_store(const f32x16& acc, char* o) {
  float g[8];
#pragma unroll
  for (int j = 0; j < 8; ++j) {
    const float x = acc[j];
    const float z = x * (1.f + 0.044715f * x * x) * (-2.f * 0.7978845608028654f * 1.4426950408889634f);
    g[j] = x * __builtin_amdgcn_rcpf(1.f + __builtin_amdgcn_exp2f(z)) * acc[8 + j];
  }
  u32x4 v = {cvtpk(g[0], g[1]), cvtpk(g[2], g[3]), cvtpk(g[4], g[5]), cvtpk(g[6], g[7])};
  st16<AUXG>(o, v);
}
DI void gu_read(const GuP& p, lds_cptr S, int k, int part, bf16x8 (&fa)[2], bf16x8 (&fb)[2]) {
  if (part == 0) { fa[0] = rd128(S + p.a0 + k * 1024); fb[0] = cat8(trrd(S + p.b0 + k * 4096), trrd(S + p.b0 + k * 4096 + 1024)); }
  if (part == 1) { fb[1] = cat8(trrd(S + p.b1 + k * 4096), trrd(S + p.b1 + k * 4096 + 1024)); fa[1] = rd128(S + p.a0 + 4096 + k * 1024); }
}
template <int MODE, int NVM>
DI void gu_stage(const GuP& p, f32x16 (&acc)[2][2], bf16x8 (&fa)[2][2], bf16x8 (&fb)[2][2], unsigned so, unsigned sn, int st) {
  const lds_cptr S = p.L + so, Sn = p.L + sn;
#pragma unroll
  for (int k = 0; k < 4; ++k) {
    if (k == 3) { asm volatile("s_waitcnt vmcnt(%0) lgkmcnt(0)\n\ts_barrier" ::"i"(NVM) : "memory"); SBAR(); }
#pragma unroll
    for (int m = 0; m < 4; ++m) {
      acc[m >> 1][m & 1] = MFMA(fb[k & 1][m & 1], fa[k & 1][m >> 1], acc[m >> 1][m & 1]);
      if (k < 3) gu_read(p, S, k + 1, m, fa[(k + 1) & 1], fb[(k + 1) & 1]);
      else if (MODE != 2) gu_read(p, Sn, 0, m, fa[0], fb[0]);
      if (MODE == 0 && k == 3 && m >= 1) gu_dma(p, m - 1, so, st + 3);
      if (MODE != 3 && k == 0 && m >= 1 && st > 0 && st < 6) gu_dma(p, m + 2, sn == 98304u ? 0u : sn + 49152u, st + 2);
      SBAR();
    }
  }
}

#define OPD 4

typedef unsigned u32x16 __attribute__((ext_vector_type(16)));
template <int I> struct IC { static constexpr int v = I; };
template <int A, int B, typename F> DI void sfor(const F& f) { if constexpr (A < B) { f(IC<A>{}); sfor<A + 1, B>(f); } }
#define GRAN_LOAD(g, off, srd) asm volatile("buffer_load_dwordx2 %0, %1, %2, 0 offen sc1" : "=v"(g) : "v"(off), "s"(srd) : "memory")
#define PIN8(a) asm volatile("" : "+v"(a[0]), "+v"(a[1]), "+v"(a[2]), "+v"(a[3]), "+v"(a[4]), "+v"(a[5]), "+v"(a[6]), "+v"(a[7]))
#define FLAG_LOAD(f, voff, soff, srd) asm volatile("buffer_load_dword %0, %1, %2, %3 offen sc1" : "=v"(f) : "v"(voff), "s"(srd), "s"(soff) : "memory")
DI float swsum32(float x, float y) { auto r = __builtin_amdgcn_permlane32_swap(__float_as_uint(x), __float_as_uint(y), false, false); return __uint_as_float(r[0]) + __uint_as_float(r[1]); }
DI float swsum16(float x, float y) { auto r = __builtin_amdgcn_permlane16_swap(__float_as_uint(x), __float_as_uint(y), false, false); return __uint_as_float(r[0]) + __uint_as_float(r[1]); }
DI void dma16s(unsigned m0v, unsigned voff, __amdgpu_buffer_rsrc_t srd, unsigned soff) {
  asm volatile("s_mov_b32 m0, %0\n\ts_nop 0\n\tbuffer_load_dwordx4 %1, %2, %3 offen sc1 lds" ::"s"(m0v), "v"(voff), "s"(srd), "s"(soff) : "memory");
}
#define KVF "sc1"
template <int WHEN> DI void kv_st(u32x4 v, unsigned voff, __amdgpu_buffer_rsrc_t srd, unsigned soff, unsigned e) {
  if constexpr (WHEN == 0) asm volatile("buffer_store_dwordx4 %0, %1, %2, %3 offen " KVF "\n\ts_nop 1" ::"v"(v), "v"(voff), "s"(srd), "s"(soff) : "memory");
  else if constexpr (WHEN == 1) asm volatile("s_cmp_eq_u32 %4, 0\n\ts_cbranch_scc1 1f\n\tbuffer_store_dwordx4 %0, %1, %2, %3 offen " KVF "\n\ts_nop 1\n1:" ::"v"(v), "v"(voff), "s"(srd), "s"(soff), "s"(e) : "memory", "scc");
  else asm volatile("s_cmp_lg_u32 %4, 0\n\ts_cbranch_scc1 1f\n\tbuffer_store_dwordx4 %0, %1, %2, %3 offen " KVF "\n\ts_nop 1\n1:" ::"v"(v), "v"(voff), "s"(srd), "s"(soff), "s"(e) : "memory", "scc");
}
#define ACQ 0
constexpr int SPIN = 1 << 18;
#define AI __attribute__((always_inline))

#define PREW 2
DI void publish(__amdgpu_buffer_rsrc_t rS, unsigned off, unsigned tag) {
  WAITBAR("vmcnt(0)");
  __builtin_amdgcn_raw_buffer_store_b32(tag, rS, threadIdx.x == 64 ? off : 0x40000000u, 0, 16);
}
#define PIPE 1
#define PSL 2
struct Pf { unsigned a, b, c, d; };
#define PIN_PF(f) asm volatile("" : "+v"(f.a), "+v"(f.b), "+v"(f.c), "+v"(f.d))
DI void await(__amdgpu_buffer_rsrc_t rS, unsigned off, int n, unsigned tag, int w, int pw, Pf& f) {
  const int lane = threadIdx.x & 63;
  const unsigned vo = lane < n ? lane * 4u : 0x40000000u;
  unsigned cnt = SPIN;
  const unsigned long long msk = (1ull << n) - 1ull;
#define PL(f) "buffer_load_dword " f ", %5, %6, %7 offen sc1\n\ts_sleep %10\n\t"
#define PC(f) "s_waitcnt vmcnt(3)\n\tv_cmp_eq_u32 vcc, %8, " f "\n\ts_nop 4\n\ts_and_b64 vcc, vcc, %9\n\ts_cmp_eq_u64 vcc, %9\n\ts_cbranch_scc1 2f\n\t"
  asm volatile("s_cmp_lg_u32 %11, %12\n\ts_cbranch_scc1 2f\n\t" PL("%0") PL("%1") PL("%2") PL("%3")
               "1:\n\t" PC("%0") PL("%0") PC("%1") PL("%1") PC("%2") PL("%2") PC("%3") PL("%3")
               "s_sub_u32 %4, %4, 1\n\ts_cbranch_scc0 1b\n2:\n\ts_barrier"
               : "=&v"(f.a), "=&v"(f.b), "=&v"(f.c), "=&v"(f.d), "+s"(cnt) : "v"(vo), "s"(rS), "s"(off), "s"(tag), "s"(msk), "i"(PSL), "s"(w), "i"(pw) : "memory", "vcc", "scc");
#undef PL
#undef PC
}
DI void gu_dma2(const GuP& p, int piece, unsigned slot, int st) {
  if (piece < 2) dma16s(p.ldsb + slot + p.w * 2048u + piece * 1024u, p.vA, p.rA, p.sA + st * 4096u + piece * 1024u);
  else gu_dma(p, piece, slot, st);
}
template <int MODE, int NVM, typename F>
DI void gu_stage2(const GuP& p, f32x16 (&acc)[2][2], bf16x8 (&fa)[2][2], bf16x8 (&fb)[2][2], unsigned so, unsigned sn, int st, const F& side) {
  const lds_cptr S = p.L + so, Sn = p.L + sn;
#pragma unroll
  for (int k = 0; k < 4; ++k) {
    if (k == 3) { asm volatile("s_waitcnt vmcnt(%0) lgkmcnt(0)\n\ts_barrier" ::"i"(NVM) : "memory"); SBAR(); }
#pragma unroll
    for (int m = 0; m < 4; ++m) {
      acc[m >> 1][m & 1] = MFMA(fb[k & 1][m & 1], fa[k & 1][m >> 1], acc[m >> 1][m & 1]);
      if (k < 3) gu_read(p, S, k + 1, m, fa[(k + 1) & 1], fb[(k + 1) & 1]);
      else if (MODE != 2) gu_read(p, Sn, 0, m, fa[0], fb[0]);
      if (MODE == 0 && k == 3 && m >= 1) gu_dma2(p, m - 1, so, st + 3);
      if (MODE != 3 && k == 0 && m >= 1 && st > 0 && st < 6) gu_dma2(p, m + 2, sn == 98304u ? 0u : sn + 49152u, st + 2);
      side(st * 16 + k * 4 + m);
      SBAR();
    }
  }
}
#define CLRX 0
#define FORCE2 0
#define NOV 0
#define WBL 0
#define NTRY 64
#define PDL 0
#define PFG 0
#define PFG2 0
#define PFD 0
DI constexpr int op_x(int t) { return (t == 12 ? 7 : 0) + (PFG && (t == PFG || t == PFG + 3) ? 1 : 0); }
DI constexpr int op_wait(int st) { int n = st + 1 - OPD >= 0 ? op_x(st + 1 - OPD) : 0; for (int t = st + 2 - OPD; t < st; ++t) n += (t + OPD < 16 ? 4 : 0) + (t >= 0 ? op_x(t) : 0); return n; }
DI void touch(unsigned m0v, unsigned voff, __amdgpu_buffer_rsrc_t srd, unsigned soff) {
  asm volatile("s_mov_b32 m0, %0\n\ts_nop 0\n\tbuffer_load_dword %1, %2, %3 offen lds" ::"s"(m0v), "v"(voff), "s"(srd), "s"(soff) : "memory");
}
DI constexpr unsigned dn_slot(int s) { return s >= 20 ? dn_slot(s - 20) : s < 6 ? s * 2048u : s < 14 ? 98304u + (s - 6) * 2048u : 49152u + (s - 14) * 2048u; }
DI constexpr bool dn_big(int s) { return s >= 20 ? dn_big(s - 20) : s >= 6 && s < 14; }
DI void tail(const u16* x, const u16* Wo, const u16* Wg, const u16* Wu, const u16* Wd, char* ws, u16* out, lds_cptr L, int w, unsigned tag PROF_REF) {
  const int tid = threadIdx.x, lane = tid & 63, h = lane >> 5, l31 = lane & 31;
  const int b = blockIdx.x, xc = b & 7, i = b >> 3;
  const unsigned ldsb = (unsigned)(size_t)L;
  const __amdgpu_buffer_rsrc_t rS = mk_srd(ws, (unsigned)WS_END);
  const int q = (lane & 15) >> 2, p4 = lane & 3, blk = (lane >> 4) & 1, pp = ((p4 & 1) << 1) | (p4 >> 1);
  const int kq = xc >> 1, rb = (xc & 1) * 8 + (i & 7), cb = i >> 3;
  const int rr = lane & 7, rc = w * 8 + (lane >> 3), rrow = rb * 128 + (kq * 4 + cb) * 8 + rr;
  const unsigned rfo = (unsigned)(((rrow >> 5) * 32 + (rc >> 1)) * 64 + (rc & 1) * 32 + (rrow & 31)) * 16u;
  u32x4 xw; f32x4 mv[6];
  const int rg = xc * 2 + (i & 1), cg = b >> 4;
  const bool gu = b < 224;
  GuP p;
  p.w = w; p.L = L; p.ldsb = ldsb;
  p.rA = rS; p.rG = mk_srd(Wg, gu ? 512u * FF * 2u : 0u); p.rU = mk_srd(Wu, gu ? 512u * FF * 2u : 0u);
  p.vA = lane * 16u;
  p.vG = (lane >> 4) * (FF * 2u) + (((lane & 15) ^ ((lane >> 4) * 4)) * 16u);
  p.vU = (lane >> 4) * (FF * 2u) + (((lane & 15) ^ ((lane >> 4) * 4) ^ 2) * 16u);
  p.sA = (unsigned)WS_H2F + (unsigned)((rg * 4 + (w >> 1)) * 32 + (w & 1) * 2) * 1024u;
  p.sW = (unsigned)(w * 8) * (FF * 2u) + cg * 256u;
  auto gu_pre = [&]() AI {
#pragma unroll
    for (int st = 0; st < 3; ++st)
#pragma unroll
      for (int k = 2; k < 6; ++k) gu_dma(p, k, st * 49152u, st);
  };
  {
    const int r = w & 3, kh = w >> 2;
    const __amdgpu_buffer_rsrc_t sW = mk_srd(Wo, 4096u * 1024u);
    const unsigned av = lane * 16u + (unsigned)WS_CF + (unsigned)((rb * 4 + r) * 256 + kq * 64 + kh * 32) * 1024u;
    const unsigned wv = (lane >> 4) * 1024u + (((lane & 15) ^ ((lane >> 4) * 4)) * 16u);
    const unsigned ws0 = (unsigned)(kq * 1024 + kh * 512 + r * 8) * 1024u + cb * 256u;
    const unsigned ld0 = ldsb + kh * 8192u + r * 2048u;
    bf16x8 a[32];
    auto issue = [&](int st) AI {
      dma16(ld0 + (st & 7) * 16384u, wv, sW, ws0 + st * 32768u);
      dma16(ld0 + (st & 7) * 16384u + 1024u, wv, sW, ws0 + st * 32768u + 4096u);
      asm volatile("buffer_load_dwordx4 %0, %2, %3, %4 offen\n\tbuffer_load_dwordx4 %1, %2, %3, %4 offen offset:1024"
                   : "=&v"(a[2 * st]), "=&v"(a[2 * st + 1]) : "v"(av), "s"(rS), "s"(st * 2048u) : "memory");
    };
#pragma unroll
    for (int st = 0; st < OPD; ++st) issue(st);
    lds_cptr rd[4];
#pragma unroll
    for (int c = 0; c < 4; ++c) rd[c] = L + kh * 8192 + (4 * h + q) * 256 + ((c ^ q) * 64 + blk * 32 + pp * 8);
    f32x16 acc[4] = {};
    bf16x8 f[2][4];
    asm volatile("s_waitcnt vmcnt(%2)\n\ts_barrier" : "+v"(a[0]), "+v"(a[1]) : "i"(4 * (OPD - 1)) : "memory");
    RSTAMP(1);
#pragma unroll
    for (int c = 0; c < 4; ++c) f[0][c] = cat8(trrd(rd[c]), trrd(rd[c] + 2048));
#pragma unroll
    for (int st = 0; st < 16; ++st) {
      const int so = (st & 7) * 16384, sn = ((st + 1) & 7) * 16384;
#pragma unroll
      for (int c = 0; c < 4; ++c) {
        acc[c] = MFMA(f[0][c], a[2 * st], acc[c]);
        f[1][c] = cat8(trrd(rd[c] + so + 4096), trrd(rd[c] + so + 4096 + 2048));
        SBAR();
      }
      if (st < 15) {
        asm volatile("s_waitcnt vmcnt(%2) lgkmcnt(0)\n\ts_barrier" : "+v"(a[2 * st + 2]), "+v"(a[2 * st + 3]) : "i"(op_wait(st)) : "memory");
      } else {
        asm volatile("s_waitcnt lgkmcnt(0)\n\ts_barrier" ::: "memory");
      }
      SBAR();
#pragma unroll
      for (int c = 0; c < 4; ++c) {
        acc[c] = MFMA(f[1][c], a[2 * st + 1], acc[c]);
        if (st < 15) f[0][c] = cat8(trrd(rd[c] + sn), trrd(rd[c] + sn + 2048));
        if (c == 0 && st + OPD < 16) issue(st + OPD);
        if (PFG && c == 0 && (st == PFG || st == PFG + 3))
          touch(ldsb + 148480u, lane < 21 ? lane * 128u : 0x40000000u, mk_srd(st == PFG ? Wg : Wu, 512u * FF * 2u), (unsigned)(i * 8 + w) * 2688u);
        if (c == 0 && st == 12) {
          asm volatile("buffer_load_dwordx4 %0, %1, %2, 0 offen" : "=v"(xw) : "v"((unsigned)(rrow * DM + rc * 8) * 2u), "s"(mk_srd(x, SEQ * DM * 2u)) : "memory");
#pragma unroll
          for (int e = 0; e < 6; ++e) asm volatile("buffer_load_dwordx4 %0, %1, %2, %3 offen sc1" : "=v"(mv[e]) : "v"(rc * 32u), "s"(rS), "s"((unsigned)WS_MOD + 4096u + (e >> 1) * 2048u + (e & 1) * 16u) : "memory");
        }
        SBAR();
      }
    }
    RSTAMP(2);
    LAS f32x4* X = (LAS f32x4*)((lds_ptr)L + w * 8192);
    const LAS f32x4* Y = (const LAS f32x4*)(L + (w ^ 4) * 8192);
    if (kh == 0) {
#pragma unroll
      for (int d = 0; d < 2; ++d)
#pragma unroll
        for (int e = 0; e < 4; ++e) X[(d * 4 + e) * 64 + lane] = (f32x4){acc[2 + d][4 * e], acc[2 + d][4 * e + 1], acc[2 + d][4 * e + 2], acc[2 + d][4 * e + 3]};
    } else {
#pragma unroll
      for (int d = 0; d < 2; ++d)
#pragma unroll
        for (int e = 0; e < 4; ++e) X[(d * 4 + e) * 64 + lane] = (f32x4){acc[d][4 * e], acc[d][4 * e + 1], acc[d][4 * e + 2], acc[d][4 * e + 3]};
      acc[0] = acc[2]; acc[1] = acc[3];
    }
    WAITBAR("lgkmcnt(0)");
    const unsigned po = lane * 16u + (unsigned)WS_PP + (unsigned)((kq * 64 + rb * 4 + r) * 32 + cb * 8 + kh * 4) * 1024u;
#pragma unroll
    for (int d = 0; d < 2; ++d)
#pragma unroll
      for (int e = 0; e < 2; ++e) {
        const f32x4 y0 = Y[(d * 4 + 2 * e) * 64 + lane], y1 = Y[(d * 4 + 2 * e + 1) * 64 + lane];
        const u32x4 o = {cvtpk(acc[d][8 * e] + y0[0], acc[d][8 * e + 1] + y0[1]), cvtpk(acc[d][8 * e + 2] + y0[2], acc[d][8 * e + 3] + y0[3]),
                         cvtpk(acc[d][8 * e + 4] + y1[0], acc[d][8 * e + 5] + y1[1]), cvtpk(acc[d][8 * e + 6] + y1[2], acc[d][8 * e + 7] + y1[3])};
        __builtin_amdgcn_raw_buffer_store_b128(o, rS, po + (d * 2 + e) * 1024u, 0, 16);
      }
    RSTAMP(3);
    asm volatile("s_barrier" ::: "memory");
    if (PREW == 2) gu_pre();
    RSTAMP(4);
  }
  {
    u32x4 pq[4];
    auto pload = [&]() AI {
      asm volatile("buffer_load_dwordx4 %0, %4, %5, %6 offen sc1\n\tbuffer_load_dwordx4 %1, %4, %5, %7 offen sc1\n\tbuffer_load_dwordx4 %2, %4, %5, %8 offen sc1\n\t"
                   "buffer_load_dwordx4 %3, %4, %5, %9 offen sc1\n\ts_waitcnt vmcnt(0)"
                   : "=&v"(pq[0]), "=&v"(pq[1]), "=&v"(pq[2]), "=&v"(pq[3])
                   : "v"(rfo), "s"(rS), "s"((unsigned)WS_PP), "s"((unsigned)WS_PP + (unsigned)(SEQ * DM * 2)), "s"((unsigned)WS_PP + (unsigned)(SEQ * DM * 4)), "s"((unsigned)WS_PP + (unsigned)(SEQ * DM * 6)) : "memory");
    };
    auto pbad = [&]() AI {
      unsigned m = 0;
#pragma unroll
      for (int k = 0; k < 4; ++k) m = max(max(m, max(pq[k][0], pq[k][1])), max(pq[k][2], pq[k][3]));
      return __any(m == 0xffffffffu);
    };
    if (PDL) __builtin_amdgcn_s_sleep(PDL);
    pload();
    __builtin_amdgcn_raw_buffer_store_b32(tag, rS, lane == 0 ? (unsigned)WS_F1 + rb * 512u + ((kq * 4 + cb) * 8 + w) * 4u : 0x40000000u, 0, 16);
    asm volatile("" : "+v"(xw), "+v"(mv[0]), "+v"(mv[1]), "+v"(mv[2]), "+v"(mv[3]), "+v"(mv[4]), "+v"(mv[5]));
    RSTAMP(5);
    int tries = 0;
#pragma unroll 1
    for (; tries < NTRY && pbad(); ++tries) pload();
    if (tries == NTRY) {
#pragma unroll 1
      for (int spin = 0; spin < SPIN; ++spin) {
        u32x2 f;
        asm volatile("buffer_load_dwordx2 %0, %1, %2, %3 offen sc1\n\ts_waitcnt vmcnt(0)" : "=v"(f) : "v"(lane * 8u), "s"(rS), "s"((unsigned)WS_F1 + rb * 512u) : "memory");
        if (__all(f[0] == tag && f[1] == tag)) break;
      }
      pload();
    }
    if (PREW != 2) gu_pre();
    RSTAMP(6);
    LAS f32x2* st = (LAS f32x2*)((lds_ptr)L + 147456);
    float v[8];
#pragma unroll
    for (int j = 0; j < 4; ++j) {
      const float ga = mv[j >> 1][2 * (j & 1)], gb = mv[j >> 1][2 * (j & 1) + 1];
      v[2 * j] = bf_lo(xw[j]) + ga * ((bf_lo(pq[0][j]) + bf_lo(pq[1][j])) + (bf_lo(pq[2][j]) + bf_lo(pq[3][j])));
      v[2 * j + 1] = bf_hi(xw[j]) + gb * ((bf_hi(pq[0][j]) + bf_hi(pq[1][j])) + (bf_hi(pq[2][j]) + bf_hi(pq[3][j])));
    }
    const u32x4 o1 = {cvtpk(v[0], v[1]), cvtpk(v[2], v[3]), cvtpk(v[4], v[5]), cvtpk(v[6], v[7])};
    __builtin_amdgcn_raw_buffer_store_b128(o1, rS, rfo, (unsigned)WS_X1F, 16);
#pragma unroll
    for (int j = 0; j < 4; ++j) { v[2 * j] = bf_lo(o1[j]); v[2 * j + 1] = bf_hi(o1[j]); }
    float mw = (((v[0] + v[1]) + (v[2] + v[3])) + ((v[4] + v[5]) + (v[6] + v[7]))) * 0.125f, m2 = 0;
#pragma unroll
    for (int j = 0; j < 8; ++j) m2 += (v[j] - mw) * (v[j] - mw);
    {
      const float mo = __int_as_float(__builtin_amdgcn_update_dpp(0, __float_as_int(mw), 0x128, 0xf, 0xf, false)), d = mo - mw;
      m2 = dppadd<0x128, 0xf>(m2) + 4.f * d * d; mw = 0.5f * (mw + mo);
      const auto r1 = __builtin_amdgcn_permlane16_swap(__float_as_uint(mw), __float_as_uint(mw), false, false);
      const float d1 = __uint_as_float(r1[0]) - __uint_as_float(r1[1]);
      m2 = swsum16(m2, m2) + 8.f * d1 * d1; mw = 0.5f * (__uint_as_float(r1[0]) + __uint_as_float(r1[1]));
      const auto r2 = __builtin_amdgcn_permlane32_swap(__float_as_uint(mw), __float_as_uint(mw), false, false);
      const float d2 = __uint_as_float(r2[0]) - __uint_as_float(r2[1]);
      m2 = swsum32(m2, m2) + 16.f * d2 * d2; mw = 0.5f * (__uint_as_float(r2[0]) + __uint_as_float(r2[1]));
    }
    if (lane < 8) st[rr * 8 + w] = (f32x2){mw, m2};
    WAITBAR("lgkmcnt(0)");
    float mu = 0;
    f32x2 t[8];
#pragma unroll
    for (int k = 0; k < 8; ++k) { t[k] = st[rr * 8 + k]; mu += t[k][0]; }
    mu *= 0.125f;
    float qv = 0;
#pragma unroll
    for (int k = 0; k < 8; ++k) qv += t[k][1] + 64.f * (t[k][0] - mu) * (t[k][0] - mu);
    const float rs = rsqrtf(qv * (1.f / DM) + 1e-5f);
    float hh[8];
#pragma unroll
    for (int j = 0; j < 4; ++j) { hh[j] = (v[j] - mu) * rs * rbf(1.f + mv[4][j]) + mv[2][j]; hh[4 + j] = (v[4 + j] - mu) * rs * rbf(1.f + mv[5][j]) + mv[3][j]; }
    const u32x4 o2 = {cvtpk(hh[0], hh[1]), cvtpk(hh[2], hh[3]), cvtpk(hh[4], hh[5]), cvtpk(hh[6], hh[7])};
    __builtin_amdgcn_raw_buffer_store_b128(o2, rS, rfo, (unsigned)WS_H2F, 16);
    RSTAMP(7);
    publish(rS, (unsigned)WS_F2 + rb * 256u + (kq * 4 + cb) * 4u, tag);
    RSTAMP(8);
  }
  const int drb = xc * 4 + (i & 3), dnb = i >> 2, dg = w & 3;
  const __amdgpu_buffer_rsrc_t sD = mk_srd(Wd, (unsigned)FF * 1024u);
  const unsigned dwv = (lane >> 3) * 1024u + (((lane & 7) ^ (6 * ((lane >> 4) & 1))) * 16u) + dnb * 128u;
  const unsigned dl0 = ldsb + dg * 12288u, dl1 = ldsb + dg * 16384u, dso = (unsigned)(dg * 28) * 16384u;
  auto dn_w = [&](int s, int half, unsigned so) AI { dma16((dn_big(s) ? dl1 : dl0) + dn_slot(s) + half * 1024u, dwv, sD, so + s * 16384u + half * 8192u); };
  auto dn_pre = [&](int n0, int n1) AI {
    const unsigned od = (unsigned)(w >> 2);
#pragma unroll
    for (int n = n0; n < n1; ++n) dma16((dn_big(n & ~1) ? dl1 : dl0) + od * 2048u + dn_slot(n & ~1) + (n & 1) * 1024u, dwv, sD, dso + od * 16384u + (n & ~1) * 16384u + (n & 1) * 8192u);
  };
  if (gu) {
    Pf pf;
    await(rS, (unsigned)WS_F2 + rg * 256u, 16, tag, w, 0, pf);
    RSTAMP(9);
#pragma unroll
    for (int st = 0; st < 3; ++st)
#pragma unroll
      for (int k = 0; k < 2; ++k) gu_dma2(p, k, st * 49152u, st);
    const int wr = w & 1, wc = w >> 1;
    p.a0 = wr * 8192u + lane * 16u;
    const unsigned bb = 16384u + blk * 16384u + (8 * h + q) * 256u;
    p.b0 = bb + ((wc * 64u + p4 * 8u) ^ (q * 64u) ^ (blk * 32u));
    p.b1 = bb + ((wc * 64u + 32u + p4 * 8u) ^ (q * 64u) ^ (blk * 32u));
    WAITBAR("vmcnt(4)");
    PIN_PF(pf);
    RSTAMP(10);
    f32x16 acc[2][2] = {};
    bf16x8 fa[2][2], fb[2][2];
    gu_read(p, p.L, 0, 0, fa[0], fb[0]); gu_read(p, p.L, 0, 1, fa[0], fb[0]);
    auto side = [&](int u) AI {
      if (u >= 93 && u < 108 && (u & 1)) dn_pre(6 + (u - 93) / 2, 7 + (u - 93) / 2);
      if (u >= 109) dn_pre(2 * (u - 109), 2 * (u - 109) + 2);
    };
    gu_stage2<0, 2>(p, acc, fa, fb, 0u, 49152u, 0, side);
    gu_stage2<0, 6>(p, acc, fa, fb, 49152u, 98304u, 1, side);
    gu_stage2<0, 6>(p, acc, fa, fb, 98304u, 0u, 2, side);
    gu_stage2<0, 6>(p, acc, fa, fb, 0u, 49152u, 3, side);
    gu_stage2<0, 6>(p, acc, fa, fb, 49152u, 98304u, 4, side);
    gu_stage2<1, 6>(p, acc, fa, fb, 98304u, 0u, 5, side);
    gu_stage2<2, 8>(p, acc, fa, fb, 0u, 49152u, 6, side);
    RSTAMP(11);
    char* const o = ws + WS_GF + ((size_t)(rg * 4 + wr * 2) * (FF / 16) + cg * 8 + wc * 2) * 1024 + lane * 16;
    const lds_cptr S = p.L + 49152;
    bf16x8 tb[2][4], ta[2][4];
#pragma unroll
    for (int k = 0; k < 4; ++k) { tb[0][k] = cat8(trrd(S + p.b0 + k * 4096), trrd(S + p.b0 + k * 4096 + 1024)); ta[0][k] = rd128(S + p.a0 + k * 1024); }
#pragma unroll
    for (int k = 0; k < 4; ++k) tb[1][k] = cat8(trrd(S + p.b1 + k * 4096), trrd(S + p.b1 + k * 4096 + 1024));
#pragma unroll
    for (int k = 0; k < 4; ++k) ta[1][k] = rd128(S + p.a0 + 4096 + k * 1024);
    SBAR();
#pragma unroll
    for (int t = 0; t < 4; ++t) {
#pragma unroll
      for (int k = 0; k < 4; ++k) acc[t >> 1][t & 1] = MFMA(tb[t & 1][k], ta[t >> 1][k], acc[t >> 1][t & 1]);
      SBAR();
      if (t > 0) gu_store(acc[(t - 1) >> 1][(t - 1) & 1], o + (size_t)((t - 1) >> 1) * (FF / 16) * 1024 + ((t - 1) & 1) * 1024);
      SBAR();
    }
    gu_store(acc[1][1], o + (size_t)(FF / 16) * 1024 + 1024);
    RSTAMP(12);
    asm volatile("s_barrier" ::: "memory");
    __builtin_amdgcn_raw_buffer_store_b32(tag, rS, tid == 64 ? (unsigned)WS_F3 + rg * 256u + cg * 4u : 0x40000000u, 0, 16);
    if (w >= 4) {
#pragma unroll
      for (int n = 0; n < 12; ++n) dn_w(14 + (n >> 1), n & 1, dso);
      asm volatile("s_waitcnt vmcnt(17)" ::: "memory");
    } else asm volatile("s_waitcnt vmcnt(5)" ::: "memory");
  } else {
    unsigned t[7] = {};
    if (PFD) {
      if (PFD > 1) __builtin_amdgcn_s_sleep(PFD);
#pragma unroll
      for (int k = 0; k < 7; ++k) asm volatile("buffer_load_dword %0, %1, %2, %3 offen" : "=v"(t[k]) : "v"(lane * 128u), "s"(sD), "s"((unsigned)(((i & 3) * 8 + w) * 7 + k) * 8192u) : "memory");
    }
    dn_pre(0, 20);
    asm volatile("s_waitcnt vmcnt(0)" : "+v"(t[0]), "+v"(t[1]), "+v"(t[2]), "+v"(t[3]), "+v"(t[4]), "+v"(t[5]), "+v"(t[6])::"memory");
  }
  RSTAMP(13);
  Pf pf;
  await(rS, (unsigned)WS_F3 + (drb >> 1) * 256u, 14, tag, w, 4, pf);
  RSTAMP(14);
  {
    LAS unsigned* vote = (LAS unsigned*)((lds_ptr)L + 98304 + 8192);
    const int rblk = drb * 2 + ((w >> 1) & 1), row = rblk * 32 + l31, n0 = dnb * 64 + (w & 1) * 32;
    u32x4 xr[2]; f32x4 gv[2][2];
    auto finish = [&](const f32x16& mine) AI {
#pragma unroll
      for (int sp = 0; sp < 2; ++sp) {
        const int n = n0 + 16 * sp + 8 * h;
        u32x4 o;
#pragma unroll
        for (int j = 0; j < 2; ++j) {
          o[j] = cvtpk(bf_lo(xr[sp][j]) + gv[sp][0][2 * j] * mine[8 * sp + 2 * j], bf_hi(xr[sp][j]) + gv[sp][0][2 * j + 1] * mine[8 * sp + 2 * j + 1]);
          o[2 + j] = cvtpk(bf_lo(xr[sp][2 + j]) + gv[sp][1][2 * j] * mine[8 * sp + 4 + 2 * j], bf_hi(xr[sp][2 + j]) + gv[sp][1][2 * j + 1] * mine[8 * sp + 4 + 2 * j + 1]);
        }
        st16<AUXO>(out + (size_t)row * DM + n, o);
      }
    };
    if (w < 4) {
      constexpr int KS = 28, D = 16, KT = 112;
      const unsigned av0 = lane * 16u + (unsigned)WS_GF + (unsigned)(drb * 2 * KT + w * KS) * 1024u, av1 = av0 + KT * 1024u;
      bf16x8 a[D][2];
      auto issue = [&](int s) AI {
        asm volatile("buffer_load_dwordx4 %0, %1, %2, %3 offen sc1" : "=v"(a[s % D][0]) : "v"(av0), "s"(rS), "s"(s * 1024u) : "memory");
        asm volatile("buffer_load_dwordx4 %0, %1, %2, %3 offen sc1" : "=v"(a[s % D][1]) : "v"(av1), "s"(rS), "s"(s * 1024u) : "memory");
      };
#pragma unroll
      for (int s = 0; s < D; ++s) issue(s);
      const unsigned swz = ((q >> 1) & 1) * 96u;
      const unsigned r0 = (4 * h + q) * 128u + ((blk * 32u + pp * 8u) ^ swz), r1 = (4 * h + q) * 128u + ((64u + blk * 32u + pp * 8u) ^ swz);
      const lds_cptr R00 = L + w * 12288 + r0, R01 = L + w * 12288 + r1, R10 = L + w * 16384 + r0, R11 = L + w * 16384 + r1;
      f32x16 acc[2][2] = {};
      unsigned mx = 0;
      bf16x8 bw[2][2];
      auto wrd = [&](int t, bf16x8 (&o)[2]) AI {
        const lds_cptr c0 = (dn_big(t) ? R10 : R00) + dn_slot(t), c1 = (dn_big(t) ? R11 : R01) + dn_slot(t);
        o[0] = cat8(trrd(c0), trrd(c0 + 1024)); o[1] = cat8(trrd(c1), trrd(c1 + 1024));
      };
      wrd(0, bw[0]);
      sfor<0, KS>([&](auto SS) AI {
        constexpr int s = decltype(SS)::v;
        constexpr auto added = [](int t) constexpr { return (t >= 1 && t <= 8 ? 2 : 0) + (t >= 1 && t <= 12 ? 2 : 0) + (t == 13 ? 6 : 0); };
        constexpr auto upto = [added](int t) constexpr { int n = 2 * D; for (int u = 0; u <= t; ++u) n += added(u); return n; };
        constexpr int posA = s < D ? 2 * (s + 1) : upto(s - 15);
        constexpr int posW = s + 1 >= 20 && s + 1 < KS ? upto(s - 18) - 2 : 0;
        constexpr int need = posA > posW ? posA : posW;
        constexpr int nvm = upto(s - 1) - need;
        asm volatile("s_waitcnt vmcnt(%2)" : "+v"(a[s % D][0]), "+v"(a[s % D][1]) : "i"(nvm) : "memory");
        if (s == 13) asm volatile("s_barrier" ::: "memory");
        if (s + 1 < KS) wrd(s + 1, bw[(s + 1) & 1]);
        const bf16x8 b0 = bw[s & 1][0], b1 = bw[s & 1][1];
        if (s >= 1 && s <= 8) { dn_w(s + 19, 0, dso); dn_w(s + 19, 1, dso); }
        if (s >= 1 && s <= 12) issue(s + 15);
        if (s == 13) {
#pragma unroll
          for (int sp = 0; sp < 2; ++sp) {
            const int n = n0 + 16 * sp + 8 * h;
            asm volatile("buffer_load_dwordx4 %0, %1, %2, %3 offen sc1" : "=v"(xr[sp]) : "v"(((unsigned)(rblk * 32 + (n >> 4)) * 64 + lane) * 16u), "s"(rS), "s"((unsigned)WS_X1F) : "memory");
            asm volatile("buffer_load_dwordx4 %0, %1, %2, %3 offen sc1" : "=v"(gv[sp][0]) : "v"(n * 4u), "s"(rS), "s"((unsigned)WS_MOD + 10240u) : "memory");
            asm volatile("buffer_load_dwordx4 %0, %1, %2, %3 offen offset:16 sc1" : "=v"(gv[sp][1]) : "v"(n * 4u), "s"(rS), "s"((unsigned)WS_MOD + 10240u) : "memory");
          }
        }
        acc[0][0] = MFMA(b0, a[s % D][0], acc[0][0]);
        acc[1][0] = MFMA(b0, a[s % D][1], acc[1][0]);
        acc[0][1] = MFMA(b1, a[s % D][0], acc[0][1]);
        acc[1][1] = MFMA(b1, a[s % D][1], acc[1][1]);
#pragma unroll
        for (int e = 0; e < 2; ++e) { const u32x4 u = __builtin_bit_cast(u32x4, a[s % D][e]); mx = max(max(mx, max(u[0], u[1])), max(u[2], u[3])); }
        SBAR();
      });
      RSTAMP(15);
      vote[w * 4096] = __any(mx == 0xffffffffu);
      LAS f32x4* X = (LAS f32x4*)((lds_ptr)L);
#pragma unroll
      for (int d = 0; d < 4; ++d) {
        if (d != w) {
#pragma unroll
          for (int g = 0; g < 4; ++g) {
            const f32x16& v = acc[d >> 1][d & 1];
            X[((w * 3 + ((d - w - 1) & 3)) * 4 + g) * 64 + lane] = (f32x4){v[4 * g], v[4 * g + 1], v[4 * g + 2], v[4 * g + 3]};
          }
        }
      }
      asm volatile("s_waitcnt vmcnt(0) lgkmcnt(0)\n\ts_barrier" : "+v"(xr[0]), "+v"(xr[1]), "+v"(gv[0][0]), "+v"(gv[0][1]), "+v"(gv[1][0]), "+v"(gv[1][1])::"memory");
      f32x16 mine = w == 0 ? acc[0][0] : w == 1 ? acc[0][1] : w == 2 ? acc[1][0] : acc[1][1];
#pragma unroll
      for (int sl = 1; sl < 4; ++sl)
#pragma unroll
        for (int g = 0; g < 4; ++g) {
          const f32x4 v = X[((((w + sl) & 3) * 3 + 3 - sl) * 4 + g) * 64 + lane];
#pragma unroll
          for (int e = 0; e < 4; ++e) mine[4 * g + e] += v[e];
        }
      finish(mine);
      if (rfl(__any(vote[(lane & 3) * 4096] != 0))) {
#pragma unroll 1
        for (int spin = 0; spin < SPIN; ++spin) {
          unsigned f;
          asm volatile("buffer_load_dword %0, %1, %2, %3 offen sc1\n\ts_waitcnt vmcnt(0)" : "=v"(f) : "v"(lane < 14 ? lane * 4u : 0u), "s"(rS), "s"((unsigned)WS_F3 + (drb >> 1) * 256u + 128u) : "memory");
          if (__all(f == tag)) break;
        }
        const u16* wp = Wd + (size_t)(4 * h) * DM + n0 + ((l31 & 19) | ((l31 & 4) << 1) | ((l31 & 8) >> 1));
        f32x16 c = {};
#pragma unroll 1
        for (int ks = 0; ks < KT; ++ks) {
          const u32x4 av = __builtin_amdgcn_raw_buffer_load_b128(rS, lane * 16u, (unsigned)WS_GF + (unsigned)(rblk * KT + ks) * 1024u, 16);
          bf16x8 bv;
#pragma unroll
          for (int e = 0; e < 8; ++e) bv[e] = (short)wp[(size_t)(ks * 16 + 8 * (e >> 2) + (e & 3)) * DM];
          c = MFMA(bv, __builtin_bit_cast(bf16x8, av), c);
        }
        finish(c);
      }
    } else {
      WAITBAR("vmcnt(0)");
      PIN_PF(pf);
      if (WBL == 1 && w == 5 && gu) asm volatile("buffer_wbl2 sc1\n\ts_waitcnt vmcnt(0)" ::: "memory");
      asm volatile("s_barrier" ::: "memory");
      if (WBL == 2 && w == 5 && gu) asm volatile("buffer_wbl2 sc1\n\ts_waitcnt vmcnt(0)" ::: "memory");
      __builtin_amdgcn_raw_buffer_store_b32(tag, rS, tid == 320 && gu ? (unsigned)WS_F3 + rg * 256u + 128u + cg * 4u : 0x40000000u, 0, 16);
    }
  }
}
__global__ __launch_bounds__(512, 2) void k_tail(const u16* __restrict__ x, const u16* __restrict__ Wo, const u16* __restrict__ Wg, const u16* __restrict__ Wu, const u16* __restrict__ Wd,
                                                 char* __restrict__ ws, u16* __restrict__ out PROF_ARG) {
  extern __shared__ __attribute__((aligned(16))) char lds[];
  STAMP_DECL
  asm volatile("" : "+s"(out));
  const int w = rfl(threadIdx.x >> 6);
  unsigned tag;
  asm volatile("s_load_dword %0, %1, %2" : "=s"(tag) : "s"(ws), "i"((int)WS_EP) : "memory");
  tail(x, Wo, Wg, Wu, Wd, ws, out, (lds_cptr)lds, w, tag PROF_FWD);
  STAMP_OUT(3, 8);
}

DI constexpr int f_late(int B) { return B == 17 || B == 19 ? 2 : B >= 16 && B <= 21 ? 1 : 0; }
DI constexpr int f_late0(int B) { int n = 0; for (int i = 16; i < B; ++i) n += f_late(i); return n; }
DI constexpr int f_w(int B) { return B == 14 ? 4 : B >= 15 && B <= 20 ? 2 : 0; }
DI constexpr int f_st(int B, bool e) { return B >= 8 && B <= 15 ? 1 : e ? (B == 16 ? 8 : 0) : f_late(B); }
#define MODLO 8
#define PFA 1
#define PFB 5
#define PFN 1
#define FPRIO 0
#define PFKS 3
#define PFC 0
#define LATE2 1
#define PFK -1
DI constexpr int f_pf(int B) { return (B >= PFA && B < PFA + PFN) + (B >= PFB && B < PFB + PFN) + (B == PFK ? 2 : 0); }
DI constexpr int f_all(int B, bool e) {
  return (B <= 13 ? 6 : B <= 21 ? 4 : B == 22 ? 6 : 0) + f_w(B) + f_st(B, e) + f_pf(B) + (B == 2 ? 2 : B == 4 || B == 6 || B == 9 || B == 12 || B == 18 || B == 20 ? 1 : 0);
}
DI constexpr int f_wait(int B, bool e) {
  if (B <= 2) return 6 + f_pf(0) + (B == 2 ? f_pf(1) : (PFK == 99 ? 2 : 0) + (PFC == 1));
  const int p = B - 2;
  const int after = B == 16 ? 3 : B == 18 && e ? 0 : B <= 15 ? f_st(p, e) + (p == 6 ? 1 : 0) + f_pf(p) : f_w(p) + f_st(p, e);
  return after + f_all(B - 1, e);
}
DI void front(const u16* A, const u16* WK, const u16* WV, const u16* x, const u16* cvec, const u16* wmod, const u16* Wq, char* ws, lds_cptr L, int w, u32x16& Q0, u32x16& Q1, unsigned& tag PROF_REF) {
  const int tid = threadIdx.x, lane = tid & 63, l31 = lane & 31, h = lane >> 5, g = w >> 2, qq = w & 3;
  const int b = blockIdx.x, head = (b & 7) * 4 + ((b >> 3) & 3), j = b >> 5, ii = b >> 3;
  const unsigned ldsb = (unsigned)(size_t)L, l16 = lane * 16u;
  const __amdgpu_buffer_rsrc_t rA = mk_srd(A, SEQ * DM * 2u), rK = mk_srd(WK, 512u * 8192u), rV = mk_srd(WV, 512u * 8192u), rW = mk_srd(Wq, 512u * 8192u), rS = mk_srd(ws, (unsigned)WS_END);
  const unsigned vA0 = (lane >> 3) * 1024u + (((lane & 7) ^ (lane >> 4)) * 16u), vA1 = vA0 ^ 64u;
  const unsigned vW = (lane >> 4) * 8192u + (((lane & 15) ^ ((lane >> 4) * 4)) * 16u);
  const unsigned sA = (unsigned)(j * 256 + w * 32) * 1024u, sW = (unsigned)(w * 8) * 8192u + head * 256u;
  constexpr unsigned SLOT = 49408u, MP = 3 * SLOT, XN = MP + 2048u, GS = XN + 8192u, DUM = GS + 2048u;
  auto dpiece = [&](int s, int q) AI {
    const unsigned slot = ldsb + (s % 3) * SLOT;
    if (q < 4) dma16(slot + w * 4096u + q * 1024u, (q & 1) ? vA1 : vA0, rA, sA + q * 8192u + (s & 7) * 128u);
    else dma16<3>(slot + 32768u + w * 2080u + (q - 4) * 1024u, vW, s < 8 ? rK : rV, sW + (unsigned)((s & 7) * 64 + (q - 4) * 4) * 8192u);
  };
  const unsigned vT = lane < 16 / PFN ? (lane >> 1) * 8192u + (lane & 1) * 128u : 0x40000000u;
  auto touch = [&](int B) AI {
    if (B == PFK) {
      asm volatile("s_mov_b32 m0, %0\n\ts_nop 0\n\tbuffer_load_dword %1, %2, %3 offen lds" ::"s"(ldsb + DUM), "v"((lane & 7) < PFKS ? 0x40000000u : (lane >> 3) * 1024u + (lane & 7) * 128u), "s"(rA), "s"((unsigned)(j * 256 + (ii & 3) * 64 + w * 8) * 1024u) : "memory");
      asm volatile("s_mov_b32 m0, %0\n\ts_nop 0\n\tbuffer_load_dword %1, %2, %3 offen lds" ::"s"(ldsb + DUM), "v"(lane < 16 ? (lane >> 1) * 8192u + (lane & 1) * 128u : 0x40000000u), "s"(rK), "s"(j < PFKS ? 0x40000000u : sW + j * 524288u) : "memory");
    }
    if (B >= PFA && B < PFA + PFN) asm volatile("s_mov_b32 m0, %0\n\ts_nop 0\n\tbuffer_load_dword %1, %2, %3 offen lds" ::"s"(ldsb + DUM), "v"(vT), "s"(rV), "s"(sW + j * 524288u + (B - PFA) * (65536u / PFN)) : "memory");
    if (B >= PFB && B < PFB + PFN) asm volatile("s_mov_b32 m0, %0\n\ts_nop 0\n\tbuffer_load_dword %1, %2, %3 offen lds" ::"s"(ldsb + DUM), "v"(vT), "s"(rW), "s"(sW + j * 524288u + (B - PFB) * (65536u / PFN)) : "memory");
  };
  auto codepf = [&]() AI {
    unsigned long long pc; unsigned n;
    asm volatile("s_getpc_b64 %0\n.Lkva_head:\n\ts_mov_b32 %1, .Lkva_tail-.Lkva_head" : "=s"(pc), "=s"(n));
    asm volatile("s_mov_b32 m0, %0\n\ts_nop 0\n\tbuffer_load_dword %1, %2, %3 offen lds" ::"s"(ldsb + DUM), "v"(lane < 2 ? lane * 128u : 0x40000000u), "s"(mk_srd((const void*)pc, n)), "s"((unsigned)(ii * 8 + w) * 256u) : "memory");
  };
  if (PFC == 2) codepf();
  STAMP(3);
#pragma unroll
  for (int q = 0; q < 6; ++q) dpiece(0, q < 2 ? q + 4 : q - 2);
  STAMP(4);
  const int im = (ii + 32 - MODLO) & 31;
  const int cb = ((b & 7) * 6 + (im >> 2)) * 4 + (im & 3);
  u32x4 xr, m0, m1; unsigned cv, ep;
  {
    const __amdgpu_buffer_rsrc_t rX = mk_srd(x, SEQ * DM * 2u), rM = mk_srd(wmod, im >= 24 ? 0u : 512u * 6144u), rC = mk_srd(cvec, 1024u);
    const unsigned mo = (unsigned)(w * 64 + (lane >> 1) * 2) * 6144u + cb * 32u + (lane & 1) * 16u;
    asm volatile("buffer_load_dwordx4 %0, %1, %2, 0 offen" : "=v"(m0) : "v"(mo), "s"(rM) : "memory");
    asm volatile("buffer_load_dwordx4 %0, %1, %2, %3 offen" : "=v"(m1) : "v"(mo), "s"(rM), "s"(6144u) : "memory");
    asm volatile("buffer_load_dword %0, %1, %2, %3 offen" : "=v"(cv) : "v"((lane >> 1) * 4u), "s"(rC), "s"(w * 128u) : "memory");
    asm volatile("buffer_load_dwordx4 %0, %1, %2, %3 offen" : "=v"(xr) : "v"(l16), "s"(rX), "s"((unsigned)(b * 8 + w) * 1024u) : "memory");
    asm volatile("buffer_load_dword %0, off, %1, %2 sc1" : "=v"(ep) : "s"(rS), "s"((unsigned)WS_EP) : "memory");
  }
#pragma unroll
  for (int q = 0; q < (LATE2 ? 6 : 12); ++q) dpiece(1 + q / 6, q % 6);
  if (!LATE2) touch(0);
  touch(99);
  if (PFC == 1) codepf();
  STAMP(6);
  SBAR();
  const unsigned early = (unsigned)(j - 2) >> 31, hb = (unsigned)head * 32u * 16384u;
  const bool isca = (cb >> 5) == 1;
  const unsigned ho = (unsigned)WS_HF + (b >> 2) * 32768u + (unsigned)(w * 8 + (lane >> 3)) * 512u + (unsigned)((b & 3) * 8 + (lane & 7)) * 16u;
  const unsigned go = (unsigned)WS_GR + w * 256u + (lane & 31) * 8u + (lane >> 5) * 2048u;
  const unsigned g1 = (w == 0 && im < 24 && cb < 64 && (lane & 7) == 0) ? (unsigned)WS_GR + (cb * 8 + (lane >> 3)) * 8u : 0x40000000u;
  const unsigned g2 = (w == 0 && im < 24 && cb >= 64 && (lane & 3) == 0) ? (unsigned)WS_MOD + (cb * 16 + (lane >> 2)) * 4u : 0x40000000u;
  const unsigned t0 = tid == 0 ? 0u : 0x40000000u;
  const unsigned hfo = (unsigned)WS_HFL + (unsigned)((g ? j : 15 - j) * 16 + qq * 4) * 4u, kfo = (unsigned)WS_KF + KFL + head * 128u;
  const unsigned hv = (unsigned)WS_HF + (unsigned)(((g ? j : 15 - j) * 4 + qq) * 32) * 1024u;
  const unsigned soK = (unsigned)WS_KF + (unsigned)((head * 64 + j * 8 + qq * 2) * 8 + g * 4) * 1024u, soV = (unsigned)WS_VF + (unsigned)((head * 128 + (j * 8 + qq * 2) * 2) * 4 + g * 2) * 1024u;
  lds_cptr aA[2][2][2], rl[2][4], rw[3][2];
  const int g4 = lane >> 4;
  {
    const int q = (lane & 15) >> 2, p4 = lane & 3;
#pragma unroll
    for (int v = 0; v < 2; ++v) {
      const int rr = v ? (lane & 3) | ((lane & 4) << 1) | ((lane & 8) >> 1) : lane & 15;
      const unsigned a0 = (qq * 64 + rr) * 128u + ((g4 ^ (rr >> 1)) * 16u);
#pragma unroll
      for (int k = 0; k < 2; ++k) { aA[v][0][k] = L + (a0 ^ (k * 64u)); aA[v][1][k] = aA[v][0][k] + 2 * SLOT; asm volatile("" : "+v"(aA[v][1][k])); }
    }
    const unsigned w0 = (8 * g4 + q) * 256u + g4 * 32u;
#pragma unroll
    for (int d = 0; d < 4; ++d) { rl[0][d] = L + (SLOT + w0 + ((d * 64u + p4 * 8u) ^ (q * 64u))); rl[1][d] = rl[0][d] + 49920; asm volatile("" : "+v"(rl[1][d])); }
#pragma unroll
    for (int c = 0; c < 2; ++c) {
      rw[0][c] = L + (32768u + w0 + ((g * 128u + c * 64u + p4 * 8u) ^ (q * 64u)));
      rw[1][c] = rw[0][c] + SLOT; rw[2][c] = rw[0][c] + 2 * SLOT; asm volatile("" : "+v"(rw[1][c]), "+v"(rw[2][c]));
    }
  }
  auto afr = [&](int r, int U) AI { const int s3 = (U >> 1) % 3; return rd128(aA[U >> 4][s3 == 2][U & 1] + (s3 == 1 ? SLOT : 0) + r * 2048); };
  auto bfr = [&](int c, int U) AI { const lds_cptr a = rw[(U >> 1) % 3][c >> 1] + (U & 1) * 8320 + (c & 1) * 32; return cat8(trrd(a), trrd(a + 1024)); };
  auto qpl = [](int c) AI { return c < 5 ? c : c - 5; };
  auto wfr = [&](int d, int t) AI { const int pl = qpl(t >> 1); const lds_cptr a = rl[pl >= 3][d >> 1] + (pl % 3) * 16640 + (t & 1) * 8320 + (d & 1) * 32; return cat8(trrd(a), trrd(a + 1024)); };
  f32x4 acc[2][4][4], qa[8][2];
  bf16x8 fa[4], fb[2][4], wf[8], wg[2], hq[32];
  const unsigned vH = (g4 >> 1) * 1024u + (g4 & 1) * 512u + (lane & 15) * 16u;
  auto pair = [](const f32x4& x, const f32x4& y, float sc_) AI {
    const auto r0 = __builtin_amdgcn_permlane16_swap(cvtpk(x[0] * sc_, x[1] * sc_), cvtpk(y[0] * sc_, y[1] * sc_), false, false);
    const auto r1 = __builtin_amdgcn_permlane16_swap(cvtpk(x[2] * sc_, x[3] * sc_), cvtpk(y[2] * sc_, y[3] * sc_), false, false);
    return (u32x4){r0[0], r1[0], r0[1], r1[1]};
  };
  auto pair1 = [](const f32x4& x, const f32x4& y) AI {
    const auto r0 = __builtin_amdgcn_permlane16_swap(cvtpk(x[0], x[1]), cvtpk(y[0], y[1]), false, false);
    const auto r1 = __builtin_amdgcn_permlane16_swap(cvtpk(x[2], x[3]), cvtpk(y[2], y[3]), false, false);
    return (u32x4){r0[0], r1[0], r0[1], r1[1]};
  };
  u32x4 kv[8], hov, sa, sc;
  u32x2 gr;
  unsigned fl;
  float t[8], a[8], s0, s1, val, mu, q;
  auto wpiece = [&](int c, int i) AI { dma16(ldsb + SLOT + qpl(c) * 16640u + w * 2080u + i * 1024u, vW, rW, sW + (unsigned)(c * 64 + i * 4) * 8192u); };
  auto hpiece = [&](int i) AI { asm volatile("buffer_load_dwordx4 %0, %1, %2, %3 offen sc1" : "=v"(hq[i]) : "v"(vH), "s"(rS), "s"(hv + (i >> 1) * 2048u + (i & 1) * 256u) : "memory"); };
  auto pack = [&](int v, int i) AI {
    if (v == 0) kv[i] = pair(acc[0][(i >> 2) * 2][i & 3], acc[0][(i >> 2) * 2 + 1][i & 3], QSCALE);
    else kv[i] = pair1(acc[1][(i >> 2) * 2 + (i & 1)][i & 2], acc[1][(i >> 2) * 2 + (i & 1)][(i & 2) + 1]);
    asm volatile("" : "+v"(kv[i]));
  };
#define KVX 0u
#define NOST 0
  const unsigned kvx = (KVX) << 10;
  auto koff = [&](int i) AI { return (NOST & 1) ? 0x40000000u : (soK + (unsigned)((i >> 2) * 8192 + ((i >> 1) & 1) * 2048 + (i & 1) * 1024)) ^ kvx; };
  auto voff = [&](int i) AI { return (NOST & 2) ? 0x40000000u : (soV + (unsigned)((i >> 2) * 8192 + ((i >> 1) & 1) * 1024 + (i & 1) * 4096)) ^ kvx; };
  auto behind = [&](int B, int sl) AI {
    const int hs = sl >> 1; const bool ev = (sl & 1) == 0;
    if (B >= 1 && B <= 13) {
      if (B == 9) { if (sl == 0) __builtin_amdgcn_raw_buffer_store_b32(tag, rS, t0, (unsigned)WS_HFL + b * 4u, 16); else if (ev && hs <= 6) dpiece(B + 2, hs - 1); }
      else if (ev && hs < 6) dpiece(B + 2, hs);
    }
    if (B == 1) {
      if (sl == 0) { asm volatile("" : "+v"(m0), "+v"(m1), "+v"(cv), "+v"(xr), "+v"(ep)); tag = rfl(ep) * 0x9E3779B1u + 0x7F4A7C15u; const float c0 = bf_lo(cv); s0 = rbf(c0 * __builtin_amdgcn_rcpf(1.f + __expf(-c0))); asm volatile("" : "+v"(s0)); }
      if (sl == 1) { const float c1 = bf_hi(cv); s1 = rbf(c1 * __builtin_amdgcn_rcpf(1.f + __expf(-c1))); asm volatile("" : "+v"(s1)); }
      if (sl >= 3 && sl < 7) {
        const int e = sl - 3;
        a[2 * e] = s0 * bf_lo(m0[e]) + s1 * bf_lo(m1[e]); a[2 * e + 1] = s0 * bf_hi(m0[e]) + s1 * bf_hi(m1[e]);
        asm volatile("" : "+v"(a[2 * e]), "+v"(a[2 * e + 1]));
      }
      if (sl == 7) { t[0] = swsum32(a[0], a[4]); t[1] = swsum32(a[1], a[5]); asm volatile("" : "+v"(t[0]), "+v"(t[1])); }
      if (sl == 8) { t[2] = swsum32(a[2], a[6]); t[3] = swsum32(a[3], a[7]); asm volatile("" : "+v"(t[2]), "+v"(t[3])); }
      if (sl == 9) { s0 = swsum16(t[0], t[2]); s1 = swsum16(t[1], t[3]); asm volatile("" : "+v"(s0), "+v"(s1)); }
      if (sl == 11) { s0 = dppadd<0x128, 0xf>(s0); s1 = dppadd<0x128, 0xf>(s1); asm volatile("" : "+v"(s0), "+v"(s1)); }
      if (sl == 12) { const float u0 = +s0, u1 = +s1; *(LAS float*)((lds_ptr)L + MP + w * 256 + (((lane & 1) * 8 + (lane >> 3)) * 4 + ((lane >> 1) & 3)) * 4) = (lane & 8) ? +u1 : +u0; }
    }
    if (B == 2) {
      if (sl == 0) {
#pragma unroll
        for (int e = 0; e < 8; ++e) t[e] = *(const LAS float*)(L + MP + e * 256 + lane * 4);
      }
      if (sl == 3) { val = ((t[0] + t[1]) + (t[2] + t[3])) + ((t[4] + t[5]) + (t[6] + t[7])); asm volatile("" : "+v"(val)); }
      if (sl == 5) { val = dppadd<0xB1, 0xf>(val); val = rbf(dppadd<0x4E, 0xf>(val)); asm volatile("" : "+v"(val)); }
      if (sl == 6) { val = isca ? rbf(1.f + val) : val; asm volatile("" : "+v"(val)); }
      if (sl == 7) {
        const float nb = __int_as_float(__builtin_amdgcn_update_dpp(0, __float_as_int(val), 0x104, 0xf, 0xf, false));
        const u32x2 gv = {cvtpk(val, nb), tag};
        __builtin_amdgcn_raw_buffer_store_b64(gv, rS, g1, 0, 16);
      }
      if (sl == 9) __builtin_amdgcn_raw_buffer_store_b32(__float_as_uint(val), rS, g2, 0, 16);
    }
    if (B == 3) {
      if (sl == 0) {
#pragma unroll
        for (int e = 0; e < 4; ++e) { t[2 * e] = bf_lo(xr[e]); t[2 * e + 1] = bf_hi(xr[e]); }
        PIN8(t);
      }
      if (sl == 1) { mu = ((t[0] + t[1]) + (t[2] + t[3])) + ((t[4] + t[5]) + (t[6] + t[7])); asm volatile("" : "+v"(mu)); }
      if (sl == 2) { mu = dppadd<0xB1, 0xf>(mu); mu = dppadd<0x4E, 0xf>(mu); mu = dppadd<0x141, 0xf>(mu); asm volatile("" : "+v"(mu)); }
      if (sl == 3) { mu = dppadd<0x140, 0xf>(mu); mu = dppadd<0x142, 0xa>(mu); mu = dppadd<0x143, 0xc>(mu); asm volatile("" : "+v"(mu)); }
      if (sl == 5) {
        mu = __int_as_float(__builtin_amdgcn_readlane(__float_as_int(mu), 63)) * (1.f / DM);
#pragma unroll
        for (int e = 0; e < 8; ++e) t[e] -= mu;
        PIN8(t);
      }
      if (sl == 6) { q = ((t[0] * t[0] + t[1] * t[1]) + (t[2] * t[2] + t[3] * t[3])) + ((t[4] * t[4] + t[5] * t[5]) + (t[6] * t[6] + t[7] * t[7])); asm volatile("" : "+v"(q)); }
      if (sl == 7) { q = dppadd<0xB1, 0xf>(q); q = dppadd<0x4E, 0xf>(q); q = dppadd<0x141, 0xf>(q); asm volatile("" : "+v"(q)); }
      if (sl == 9) { q = dppadd<0x140, 0xf>(q); q = dppadd<0x142, 0xa>(q); q = dppadd<0x143, 0xc>(q); asm volatile("" : "+v"(q)); }
      if (sl == 11) { q = rsqrtf(__int_as_float(__builtin_amdgcn_readlane(__float_as_int(q), 63)) * (1.f / DM) + 1e-5f); asm volatile("" : "+v"(q)); }
      if (sl == 13) { xr[0] = cvtpk(t[0] * q, t[1] * q); xr[1] = cvtpk(t[2] * q, t[3] * q); }
      if (sl == 14) { xr[2] = cvtpk(t[4] * q, t[5] * q); xr[3] = cvtpk(t[6] * q, t[7] * q); *(LAS u32x4*)((lds_ptr)L + XN + lane * 128 + ((w ^ (lane & 7)) * 16)) = xr; }
    }
    if (B == 4 && sl == 14) GRAN_LOAD(gr, go, rS);
    if (B == 6) {
      if (sl == 0) {
        *(LAS unsigned*)((lds_ptr)L + GS + w * 256 + lane * 4) = gr[0];
        xr = *(const LAS u32x4*)(L + XN + w * 1024 + (lane >> 3) * 128 + (((lane ^ (lane >> 3)) & 7) * 16));
      }
      if (sl == 1) { sa = *(const LAS u32x4*)(L + GS + w * 256 + (lane >> 3) * 16); sc = *(const LAS u32x4*)(L + GS + 128 + w * 256 + (lane >> 3) * 16); }
      if (sl >= 5 && sl < 12 && !ev) {
        const int e = (sl - 5) >> 1;
        hov[e] = cvtpk(bf_lo(xr[e]) * bf_lo(sc[e]) + bf_lo(sa[e]), bf_hi(xr[e]) * bf_hi(sc[e]) + bf_hi(sa[e]));
        asm volatile("" : "+v"(hov[e]));
      }
      if (sl == 12) __builtin_amdgcn_raw_buffer_store_b128(hov, rS, ho, 0, 16);
    }
    if (B == 8 && sl >= 6 && sl < 14) pack(0, sl - 6);
    if (B == 12 && sl == 12) FLAG_LOAD(fl, (lane & 3) * 4u, hfo, rS);
    if (B >= 8 && B <= 15 && sl == (B == 14 ? 15 : 14)) kv_st<0>(kv[B - 8], l16, rS, koff(B - 8), early);
    if (B == 14) { if (ev && hs < 4) hpiece(hs); if (sl == 8 || sl == 10 || sl == 12 || sl == 13) wpiece(sl >= 12, sl == 10 || sl == 13); }
    if (B == 16) {
      if (sl < 4) hpiece(8 + sl);
      if (sl == 4 || sl == 5) wpiece(3, sl - 4);
      if (sl >= 6 && sl < 14) { pack(1, sl - 6); kv_st<1>(kv[sl - 6], l16, rS, voff(sl - 6), early); }
      if (sl == 15) kv_st<2>(kv[0], l16, rS, voff(0), early);
    }
    if (B == 15 || (B >= 17 && B <= 21)) {
      const int pre = B == 18 || B == 20;
      if (pre && sl == 0) {
        if (B == 18) __builtin_amdgcn_raw_buffer_store_b32(tag, rS, t0, early ? kfo + j * 4u : 0x40000000u, 16);
        else FLAG_LOAD(fl, 0u, kfo, rS);
      }
      const int i = pre ? ((sl & 1) ? hs : -1) : (ev ? hs : -1);
      if (i >= 0 && i < 4) hpiece(4 * (B - 14) + i);
      if (i >= 4 && i < 4 + f_w(B)) wpiece(B - 13, i - 4);
      if (B >= 17 && i >= 4 + f_w(B) && i < 4 + f_w(B) + f_late(B)) { const int e = f_late0(B) + i - 4 - f_w(B); kv_st<2>(kv[e], l16, rS, voff(e), early); }
    }
    if (LATE2 && B == 0 && sl >= 6 && sl < 15 && (ev || sl == 13)) dpiece(2, sl < 13 ? hs - 3 : sl - 9);
    if (B >= (LATE2 ? 0 : 1) && B <= 13 && sl == 15) touch(B);
    if (B == 22 && ev && hs < 6)
      dma16s(ldsb + (hs >> 1) * 16384u + w * 2048u + (hs & 1) * 1024u, l16, rS, (unsigned)((hs >> 1) == 1 ? WS_VF : WS_KF) + hb + (hs >> 2) * 16384u + w * 2048u + (hs & 1) * 1024u);
  };
  auto bar = [&](int B) AI {
    const int ne = f_wait(B, true), nl = f_wait(B, false), c = B >= 16 ? B - 16 : 0;
    if (B < 16) asm volatile("s_waitcnt vmcnt(%0) lgkmcnt(0)\n\ts_barrier" ::"i"(nl) : "memory");
    else if (ne <= nl) asm volatile("s_cmp_eq_u32 %4, 0\n\ts_cbranch_scc1 1f\n\ts_waitcnt vmcnt(%5)\n1:\n\ts_waitcnt vmcnt(%6) lgkmcnt(0)\n\ts_barrier"
        : "+v"(hq[4 * c]), "+v"(hq[4 * c + 1]), "+v"(hq[4 * c + 2]), "+v"(hq[4 * c + 3]) : "s"(early), "i"(ne), "i"(nl) : "memory", "scc");
    else asm volatile("s_cmp_lg_u32 %4, 0\n\ts_cbranch_scc1 1f\n\ts_waitcnt vmcnt(%6)\n1:\n\ts_waitcnt vmcnt(%5) lgkmcnt(0)\n\ts_barrier"
        : "+v"(hq[4 * c]), "+v"(hq[4 * c + 1]), "+v"(hq[4 * c + 2]), "+v"(hq[4 * c + 3]) : "s"(early), "i"(ne), "i"(nl) : "memory", "scc");
    if (B == 6) {
      asm volatile("" : "+v"(gr));
#pragma unroll 1
      for (int spin = 0; spin < SPIN; ++spin) {
        if (__all(gr[1] == tag)) break;
        GRAN_LOAD(gr, go, rS);
        asm volatile("s_waitcnt vmcnt(0)" : "+v"(gr)::"memory");
      }
    }
    if (B == 14) {
      asm volatile("" : "+v"(fl));
#pragma unroll 1
      for (int spin = 0; spin < SPIN; ++spin) {
        if (__all(fl == tag)) break;
        FLAG_LOAD(fl, (lane & 3) * 4u, hfo, rS);
        asm volatile("s_waitcnt vmcnt(0)" : "+v"(fl)::"memory");
      }
    }
    if (B == 22) {
      asm volatile("" : "+v"(fl));
#pragma unroll 1
      for (int spin = 0; spin < SPIN; ++spin) {
        if (rfl(fl) == tag) break;
        FLAG_LOAD(fl, 0u, kfo, rS);
        asm volatile("s_waitcnt vmcnt(0)" : "+v"(fl)::"memory");
      }
      if (ACQ) asm volatile("buffer_inv sc1" ::: "memory");
    }
    SBAR();
  };
  asm volatile("s_waitcnt vmcnt(%0)\n\ts_barrier" ::"i"((LATE2 ? 11 : 17 + f_pf(0)) + (PFK == 99 ? 2 : 0) + (PFC == 1)) : "memory");
  STAMP(1);
#pragma unroll
  for (int r = 0; r < 4; ++r) { fa[r] = afr(r, 0); fb[0][r] = bfr(r, 0); }
  sfor<0, 46>([&](auto UU) AI {
    constexpr int U = decltype(UU)::v, t = U - 32;
    if (U == 16) STAMP(5);
    if (U == 32) STAMP(9);
#pragma unroll
    for (int n = 0; n < 16; ++n) {
      const bool br = (U & 1) && n == 3;
      if constexpr (U < 32) {
        f32x4& c = acc[U >> 4][n >> 2][n & 3];
        if (U < 16) mfma16(c, fb[U & 1][n & 3], fa[n >> 2], U == 0); else mfma16(c, fa[n >> 2], fb[U & 1][n & 3], U == 16);
        SBAR();
        if (br) { behind(U >> 1, 15); SBAR(); bar((U + 1) >> 1); }
        if (U < 31) {
          if ((n & 3) == 3) fa[n >> 2] = afr(n >> 2, U + 1);
          if (n >= 4 && n < 12 && (n & 1) == 0) fb[(U + 1) & 1][(n - 4) >> 1] = bfr((n - 4) >> 1, U + 1);
        } else if (n >= 3 && n < 11) wf[n - 3] = wfr(n - 3, 0);
      } else {
        mfma16(qa[n >> 1][n & 1], wf[n >> 1], hq[t < 0 ? 0 : 2 * t + (n & 1)], t == 0);
        SBAR();
        if (br) { behind(U >> 1, 15); SBAR(); bar((U + 1) >> 1); wf[0] = wfr(0, t + 1); }
        if ((n & 1) && !((U & 1) && n == 1)) wf[n >> 1] = wfr(n >> 1, t + 1);
        if (t == 13 && n == 14) wg[0] = wfr(0, 15);
      }
      if ((n & 1) && !br) {
        const int p = (U & 1) * 8 + (n >> 1) - 10;
        behind((U >> 1) + (p >= 0), p >= 0 ? p : p + 16);
      }
      SBAR();
    }
  });
  const lds_ptr stash = (lds_ptr)L + (g == 0 ? 131072 + qq * 8192 : 99328) + l16;
  auto fin = [&](int d) AI {
    const u32x4 v = pair1(qa[d][0], qa[d][1]);
#pragma unroll
    for (int e = 0; e < 4; ++e) { if (d < 4) Q0[4 * d + e] = v[e]; else Q1[4 * (d - 4) + e] = v[e]; }
    *(LAS u32x4*)(stash + d * 1024) = v;
  };
#pragma unroll
  for (int d = 0; d < 8; ++d)
#pragma unroll
    for (int k = 0; k < 4; ++k) {
      if (k < 2) mfma16(qa[d][k & 1], wf[d], hq[28 + k], false); else mfma16(qa[d][k & 1], wg[d & 1], hq[28 + k], false);
      SBAR();
      if (k == 0 && d < 7) wg[(d + 1) & 1] = wfr(d + 1, 15);
      if (k == 2 && d > 0) fin(d - 1);
      SBAR();
    }
  asm volatile("s_nop 7\n\ts_nop 7" : "+v"(qa[7][0]), "+v"(qa[7][1]));
  fin(7);
  if (FPRIO) __builtin_amdgcn_s_setprio(0);
}

#define PRIO 1
#define STAMPL(i)
#define STAMPF(k)
#define MX3(a, b, c) __builtin_fmaxf(__builtin_fmaxf((a), (b)), (c))
constexpr int NW = 8;
constexpr float SUMTHR = 256.f;
#define CLOB_V "v64", "v65", "v66", "v67", "v68", "v69", "v70", "v71", "v72", "v73", "v74", "v75", "v76", "v77", "v78", "v79", "v80", "v81", "v82", "v83", "v84", "v85", "v86", "v87", "v88", "v89", "v90", "v91", "v92", "v93", "v94", "v95", "v96", "v97", "v98", "v99", "v100", "v101", "v102", "v103", "v104", "v105", "v106", "v107", "v108", "v109", "v110", "v111", "v112", "v113", "v114", "v115", "v116", "v117", "v118", "v119", "v120", "v121", "v122", "v123", "v124", "v125", "v126", "v127", "v160", "v161", "v162", "v163", "v164", "v165", "v166", "v167", "v168", "v169", "v170", "v171", "v172", "v173", "v174", "v175", "v176", "v177", "v178", "v179", "v180", "v181", "v182", "v183", "v184", "v185", "v186", "v187", "v188", "v189", "v190", "v191", "v192", "v193", "v194", "v195", "v196", "v197", "v198", "v199", "v200", "v201", "v202", "v203", "v204", "v205", "v206", "v207", "v210", "v211", "v212", "v218", "v219", "v223", "v225"
#define CLOB_S "s40", "s41", "s42", "s43", "s44", "s45", "s46", "s47", "s48", "s49", "s50", "s51", "s52", "s53", "s54", "s55", "s56", "s57", "s58", "s59", "s60", "s61", "s62", "s63", "s64", "s65", "s66", "s67", "s68", "s69", "s70", "s71", "s72", "s73", "s74", "s75", "s76", "s77", "s78", "s79", "s80", "s81", "s82", "s83", "s84", "s85", "s86", "s87", "s88", "s89", "s90", "s91", "s92", "s93", "s94", "s95"
__global__ __launch_bounds__(512, 2) void k_kva(const u16* __restrict__ A, const u16* __restrict__ WK, const u16* __restrict__ WV, const u16* __restrict__ x, const u16* __restrict__ cvec,
                                                const u16* __restrict__ wmod, const u16* __restrict__ Wq, const u16* __restrict__ Wo, const u16* __restrict__ Wg, const u16* __restrict__ Wu,
                                                const u16* __restrict__ Wd, char* __restrict__ ws PROF_ARG) {
  extern __shared__ __attribute__((aligned(16))) char lds[];
  STAMP_DECL STAMP(0);
  asm volatile("" ::"s"(x), "s"(cvec), "s"(wmod), "s"(Wq), "s"(Wo), "s"(Wg), "s"(Wu), "s"(Wd), "s"(ws));
  const int tid = threadIdx.x, lane = tid & 63, w = rfl(tid >> 6), l31 = lane & 31, h = lane >> 5, g = w >> 2, qq = w & 3;
  const int b = blockIdx.x, head = (b & 7) * 4 + ((b >> 3) & 3), j = b >> 5, p = 2 * j + 2;
  const unsigned ldsb = (unsigned)(size_t)lds;
  const unsigned hb = (unsigned)head * 32u * 16384u;
  const unsigned l16 = lane * 16u;
  const u16* pw; unsigned pwn;
  {
    const int pt = (b >> 3) * 8 + w, pu = (b & 7) * 128 + pt - 128;
    pw = pt < 128 ? Wo + (size_t)(((b & 7) >> 1) * 128 + pt) * 4096 : pu < 224 ? Wg + (size_t)pu * 4096 : pu < 448 ? Wu + (size_t)(pu - 224) * 4096 : Wd + (size_t)(pu < 672 ? pu - 448 : 0) * 4096;
    pwn = (pt < 128 || pu < 672) ? 8192u : 0u;
    asm volatile("" : "+s"(pw), "+s"(pwn));
  }
  u32x16 Q0, Q1;
  unsigned tag;
  front(A, WK, WV, x, cvec, wmod, Wq, ws, (lds_cptr)lds, w, Q0, Q1, tag PROF_FWD);
  const __amdgpu_buffer_rsrc_t sk = mk_srd(ws + WS_KF, KFL + 8192u), sv = mk_srd(ws + WS_VF, (unsigned)SEQ * NH * HD * 2u);
  const __amdgpu_buffer_rsrc_t sc = mk_srd(ws + WS_CF, (unsigned)(WS_END - WS_CF));
  const unsigned crb = g ? 15 - j : j;
  const unsigned cpp = (unsigned)(WS_PP - WS_CF) + (unsigned)qq * (unsigned)(SEQ * DM * 2) + crb * 131072u + head * 4096u - qq * 4096u;
  const unsigned cgo = (unsigned)(WS_GF - WS_CF) + crb * 458752u + head * 14336u + qq * 1024u - 16384u - qq * 4096u, cm = qq < 2 ? 0xffu : 0x7fu;
  const unsigned lowp = (1u << p) - 1u;
  const unsigned mK = 0x7fffu & (g == 0 ? ~0u : ~(lowp >> 2));
  const unsigned mV = 0xffffu & (g == 1 ? ~0u : ~(lowp >> 1));
  const unsigned mD = 0x10000u | (g == 1 ? 3u << (p - 2) : 0u);
  const unsigned mF = 1u | (g == 1 ? 1u << p : 0u);
  const unsigned mQ = g == 1 ? 1u << (p - 1) : 0u;
  const __amdgpu_buffer_rsrc_t spf = mk_srd(pw, pwn);
  const unsigned pfo = lane < 4 ? lane * 128u : 0x40000000u;
  f32x16 O[4];
  float m, l;
  u32x4 hf4;
  WAITBAR("vmcnt(0) lgkmcnt(0)");
  STAMP(2);
  __builtin_amdgcn_raw_buffer_store_b32(tag, sk, tid == 0 ? 0u : 0x40000000u, j < 2 ? 0x40000000u : KFL + head * 128u + j * 4u, 16);
  if (g == 1) asm volatile("s_barrier" ::: "memory");
  asm volatile(
      "ds_read_b128 v[160:163], v214 offset:0\n\t"
      "ds_read_b128 v[164:167], v214 offset:8192\n\t"
      "ds_read_b128 v[168:171], v214 offset:1024\n\t"
      "ds_read_b128 v[172:175], v214 offset:9216\n\t"
      "ds_read_b128 v[176:179], v214 offset:2048\n\t"
      "ds_read_b128 v[180:183], v214 offset:10240\n\t"
      "ds_read_b128 v[184:187], v214 offset:3072\n\t"
      "ds_read_b128 v[188:191], v214 offset:11264\n\t"
      "v_mov_b64 v[96:97], 0\n\t"
      "v_mov_b64 v[98:99], 0\n\t"
      "v_mov_b64 v[100:101], 0\n\t"
      "v_mov_b64 v[102:103], 0\n\t"
      "v_mov_b64 v[104:105], 0\n\t"
      "v_mov_b64 v[106:107], 0\n\t"
      "v_mov_b64 v[108:109], 0\n\t"
      "v_mov_b64 v[110:111], 0\n\t"
      "v_mov_b32 v210, v214\n\t"
      "v_mov_b32 v211, v214\n\t"
      "s_mov_b32 s40, 1\n\t"
      "s_mov_b32 s41, 2\n\t"
      "s_mov_b32 s42, %[y2]\n\t"
      "s_mov_b32 s43, 0\n\t"
      "s_mov_b32 s44, 0x8000\n\t"
      "s_add_u32 s46, %[hb], 0x4000\n\t"
      "s_add_u32 s57, %[dbase], 0x4000\n\t"
      "s_or_b32 s60, %[md], %[mf]\n\t"
      "s_mov_b32 s61, -512\n\t"
      "s_or_b32 s60, s60, 15\n\t"
      "v_mov_b64 v[232:233], -1\n\t"
      "v_mov_b64 v[234:235], -1\n\t"
      ".Lloop%=:\n\t"
      "s_barrier\n\t"
      "s_setprio 1\n\t"
      "s_waitcnt lgkmcnt(7)\n\t"
      "v_mfma_f32_32x32x16_bf16 v[64:79], v[160:163], v[128:131], v[96:111]\n\t"
      "ds_read_b128 v[160:163], v210 offset:4096\n\t"
      "s_waitcnt lgkmcnt(7)\n\t"
      "v_mfma_f32_32x32x16_bf16 v[80:95], v[164:167], v[128:131], v[96:111]\n\t"
      "ds_read_b128 v[164:167], v210 offset:12288\n\t"
      "s_waitcnt lgkmcnt(7)\n\t"
      "v_mfma_f32_32x32x16_bf16 v[64:79], v[168:171], v[132:135], v[64:79]\n\t"
      "ds_read_b128 v[168:171], v210 offset:5120\n\t"
      "s_waitcnt lgkmcnt(7)\n\t"
      "v_mfma_f32_32x32x16_bf16 v[80:95], v[172:175], v[132:135], v[80:95]\n\t"
      "ds_read_b128 v[172:175], v210 offset:13312\n\t"
      "s_waitcnt lgkmcnt(7)\n\t"
      "v_mfma_f32_32x32x16_bf16 v[64:79], v[176:179], v[136:139], v[64:79]\n\t"
      "ds_read_b128 v[176:179], v210 offset:6144\n\t"
      "s_waitcnt lgkmcnt(7)\n\t"
      "v_mfma_f32_32x32x16_bf16 v[80:95], v[180:183], v[136:139], v[80:95]\n\t"
      "ds_read_b128 v[180:183], v210 offset:14336\n\t"
      "s_waitcnt lgkmcnt(7)\n\t"
      "v_mfma_f32_32x32x16_bf16 v[64:79], v[184:187], v[140:143], v[64:79]\n\t"
      "ds_read_b128 v[184:187], v210 offset:7168\n\t"
      "s_waitcnt lgkmcnt(7)\n\t"
      "v_mfma_f32_32x32x16_bf16 v[80:95], v[188:191], v[140:143], v[80:95]\n\t"
      "ds_read_b128 v[188:191], v210 offset:15360\n\t"
      "s_waitcnt lgkmcnt(7)\n\t"
      "v_mfma_f32_32x32x16_bf16 v[64:79], v[160:163], v[144:147], v[64:79]\n\t"
      "ds_read_b128 v[160:163], v211 offset:16384\n\t"
      "s_max_i32 s54, s41, s42\n\t"
      "s_waitcnt lgkmcnt(7)\n\t"
      "v_mfma_f32_32x32x16_bf16 v[80:95], v[164:167], v[144:147], v[80:95]\n\t"
      "ds_read_b128 v[164:167], v211 offset:17408\n\t"
      "s_lshl_b32 s47, s54, 14\n\t"
      "s_waitcnt lgkmcnt(7)\n\t"
      "v_mfma_f32_32x32x16_bf16 v[64:79], v[168:171], v[148:151], v[64:79]\n\t"
      "ds_read_b128 v[168:171], v211 offset:18432\n\t"
      "s_lshl_b32 s45, s54, 15\n\t"
      "s_waitcnt lgkmcnt(7)\n\t"
      "v_mfma_f32_32x32x16_bf16 v[80:95], v[172:175], v[148:151], v[80:95]\n\t"
      "ds_read_b128 v[172:175], v211 offset:19456\n\t"
      "s_and_b32 s45, s45, 0x18000\n\t"
      "s_waitcnt lgkmcnt(7)\n\t"
      "v_mfma_f32_32x32x16_bf16 v[64:79], v[176:179], v[152:155], v[64:79]\n\t"
      "ds_read_b128 v[176:179], v211 offset:20480\n\t"
      "s_add_u32 s47, s47, %[hb]\n\t"
      "s_waitcnt lgkmcnt(7)\n\t"
      "v_mfma_f32_32x32x16_bf16 v[80:95], v[180:183], v[152:155], v[80:95]\n\t"
      "ds_read_b128 v[180:183], v211 offset:21504\n\t"
      "s_add_u32 s48, s45, %[dbase]\n\t"
      "s_waitcnt lgkmcnt(7)\n\t"
      "v_mfma_f32_32x32x16_bf16 v[64:79], v[184:187], v[156:159], v[64:79]\n\t"
      "ds_read_b128 v[184:187], v211 offset:22528\n\t"
      "s_and_b32 s53, %[mk], s40\n\t"
      "s_cselect_b32 s48, s48, %[dum]\n\t"
      "s_cselect_b32 s49, s47, 2.0\n\t"
      "s_waitcnt lgkmcnt(7)\n\t"
      "v_mfma_f32_32x32x16_bf16 v[80:95], v[188:191], v[156:159], v[80:95]\n\t"
      "ds_read_b128 v[188:191], v211 offset:23552\n\t"
      "s_add_u32 s50, s44, s57\n\t"
      "s_waitcnt lgkmcnt(7)\n\t"
      "v_mfma_f32_32x32x16_bf16 v[0:15], v[160:163], v[112:115], v[0:15]\n\t"
      "ds_read_b128 v[160:163], v211 offset:24576\n\t"
      "s_and_b32 s53, %[mv], s40\n\t"
      "s_cselect_b32 s50, s50, %[dum]\n\t"
      "s_cselect_b32 s51, s46, 2.0\n\t"
      "s_waitcnt lgkmcnt(7)\n\t"
      "v_mfma_f32_32x32x16_bf16 v[16:31], v[164:167], v[112:115], v[16:31]\n\t"
      "ds_read_b128 v[164:167], v211 offset:25600\n\t"
      "s_add_u32 s53, s43, 0x4000\n\t"
      "s_waitcnt lgkmcnt(7)\n\t"
      "v_mfma_f32_32x32x16_bf16 v[32:47], v[168:171], v[112:115], v[32:47]\n\t"
      "ds_read_b128 v[168:171], v211 offset:26624\n\t"
      "s_cmp_eq_u32 s40, 0x10000\n\t"
      "s_cselect_b32 s53, s53, s44\n\t"
      "v_add_u32 v212, s53, v214\n\t"
      "s_waitcnt lgkmcnt(7)\n\t"
      "v_mfma_f32_32x32x16_bf16 v[48:63], v[172:175], v[112:115], v[48:63]\n\t"
      "ds_read_b128 v[172:175], v211 offset:27648\n\t"
      "s_and_b32 s55, %[mf], s40\n\t"
      "s_cselect_b32 s58, 0x21800000, 0\n\t"
      "s_cselect_b32 s56, 0xf149f2ca, 0\n\t"
      "s_waitcnt lgkmcnt(7)\n\t"
      "v_mfma_f32_32x32x16_bf16 v[0:15], v[176:179], v[116:119], v[0:15]\n\t"
      "ds_read_b128 v[176:179], v211 offset:28672\n\t"
      "s_sub_u32 s59, 0x4b800000, s58\n\t"
      "s_waitcnt lgkmcnt(7)\n\t"
      "v_mfma_f32_32x32x16_bf16 v[16:31], v[180:183], v[116:119], v[16:31]\n\t"
      "ds_read_b128 v[180:183], v211 offset:29696\n\t"
      "s_and_b32 s52, s60, s40\n\t"
      "s_waitcnt lgkmcnt(7)\n\t"
      "v_mfma_f32_32x32x16_bf16 v[32:47], v[184:187], v[116:119], v[32:47]\n\t"
      "ds_read_b128 v[184:187], v211 offset:30720\n\t"
      "s_add_u32 s41, s41, 1\n\t"
      "s_waitcnt lgkmcnt(7)\n\t"
      "v_mfma_f32_32x32x16_bf16 v[48:63], v[188:191], v[116:119], v[48:63]\n\t"
      "ds_read_b128 v[188:191], v211 offset:31744\n\t"
      "s_add_u32 s42, s42, 2\n\t"
      "s_waitcnt lgkmcnt(7)\n\t"
      "v_mfma_f32_32x32x16_bf16 v[0:15], v[160:163], v[120:123], v[0:15]\n\t"
      "s_add_u32 s61, s61, 512\n\t"
      "s_waitcnt lgkmcnt(6)\n\t"
      "v_mfma_f32_32x32x16_bf16 v[16:31], v[164:167], v[120:123], v[16:31]\n\t"
      "buffer_load_dword v223, v224, %[spf], s61 offen\n\t"
      "s_waitcnt lgkmcnt(5)\n\t"
      "v_mfma_f32_32x32x16_bf16 v[32:47], v[168:171], v[120:123], v[32:47]\n\t"
      "s_lshl1_add_u32 s62, s61, %[cpp]\n\t"
      "s_waitcnt lgkmcnt(4)\n\t"
      "v_mfma_f32_32x32x16_bf16 v[48:63], v[172:175], v[120:123], v[48:63]\n\t"
      "s_lshl3_add_u32 s63, s61, %[cg]\n\t"
      "s_cmp_lt_u32 s40, 16\n\t"
      "s_cselect_b32 s62, s62, s63\n\t"
      "s_waitcnt lgkmcnt(3)\n\t"
      "v_mfma_f32_32x32x16_bf16 v[0:15], v[176:179], v[124:127], v[0:15]\n\t"
      "s_and_b32 s63, s40, %[cm]\n\t"
      "s_cselect_b32 s62, s62, 2.0\n\t"
      "s_waitcnt lgkmcnt(2)\n\t"
      "v_mfma_f32_32x32x16_bf16 v[16:31], v[180:183], v[124:127], v[16:31]\n\t"
      "buffer_store_dwordx4 v[232:235], v213, %[sc], s62 offen sc1\n\t"
      "s_waitcnt lgkmcnt(1)\n\t"
      "v_mfma_f32_32x32x16_bf16 v[32:47], v[184:187], v[124:127], v[32:47]\n\t"
      "s_waitcnt lgkmcnt(0)\n\t"
      "v_mfma_f32_32x32x16_bf16 v[48:63], v[188:191], v[124:127], v[48:63]\n\t"
      "s_setprio 0\n\t"
      "s_waitcnt vmcnt(2)\n\t"
      "s_barrier\n\t"
      "s_cmp_lg_u32 s52, 0\n\t"
      "s_cbranch_scc1 .Lspec%=\n\t"
      ".Lret%=:\n\t"
      "s_mov_b32 m0, s48\n\t"
      "ds_read_b128 v[160:163], v212 offset:0\n\t"
      "buffer_load_dwordx4 v213, %[sk], s49 offen sc1 lds\n\t"
      "v_exp_f32 v192, v64\n\t"
      "v_exp_f32 v193, v65\n\t"
      "buffer_load_dwordx4 v213, %[sk], s49 offen offset:1024 sc1 lds\n\t"
      "v_exp_f32 v194, v80\n\t"
      "v_exp_f32 v195, v81\n\t"
      "buffer_load_dwordx4 v213, %[sk], s49 offen offset:2048 sc1 lds\n\t"
      "v_exp_f32 v196, v66\n\t"
      "v_exp_f32 v197, v67\n\t"
      "buffer_load_dwordx4 v213, %[sk], s49 offen offset:3072 sc1 lds\n\t"
      "v_exp_f32 v198, v82\n\t"
      "v_exp_f32 v199, v83\n\t"
      "s_mov_b32 m0, s50\n\t"
      "ds_read_b128 v[164:167], v212 offset:8192\n\t"
      "v_cvt_pk_bf16_f32 v112, v192, v193\n\t"
      "v_cvt_pk_bf16_f32 v120, v194, v195\n\t"
      "buffer_load_dwordx4 v213, %[sv], s51 offen sc1 lds\n\t"
      "v_exp_f32 v200, v68\n\t"
      "v_exp_f32 v201, v69\n\t"
      "buffer_load_dwordx4 v213, %[sv], s51 offen offset:1024 sc1 lds\n\t"
      "v_exp_f32 v202, v84\n\t"
      "v_exp_f32 v203, v85\n\t"
      "buffer_load_dwordx4 v213, %[sv], s51 offen offset:2048 sc1 lds\n\t"
      "v_cvt_pk_bf16_f32 v113, v196, v197\n\t"
      "v_cvt_pk_bf16_f32 v121, v198, v199\n\t"
      "buffer_load_dwordx4 v213, %[sv], s51 offen offset:3072 sc1 lds\n\t"
      "v_add_f32 v192, v192, v196\n\t"
      "v_add_f32 v193, v193, v197\n\t"
      "ds_read_b128 v[168:171], v212 offset:1024\n\t"
      "v_add_f32 v194, v194, v198\n\t"
      "v_add_f32 v195, v195, v199\n\t"
      "ds_read_b128 v[172:175], v212 offset:9216\n\t"
      "v_exp_f32 v196, v70\n\t"
      "v_exp_f32 v197, v71\n\t"
      "ds_read_b128 v[176:179], v212 offset:2048\n\t"
      "v_exp_f32 v198, v86\n\t"
      "v_exp_f32 v199, v87\n\t"
      "ds_read_b128 v[180:183], v212 offset:10240\n\t"
      "v_cvt_pk_bf16_f32 v114, v200, v201\n\t"
      "v_cvt_pk_bf16_f32 v122, v202, v203\n\t"
      "ds_read_b128 v[184:187], v212 offset:3072\n\t"
      "v_add_f32 v192, v192, v200\n\t"
      "v_add_f32 v193, v193, v201\n\t"
      "ds_read_b128 v[188:191], v212 offset:11264\n\t"
      "v_add_f32 v194, v194, v202\n\t"
      "v_add_f32 v195, v195, v203\n\t"
      "v_exp_f32 v200, v72\n\t"
      "v_exp_f32 v201, v73\n\t"
      "v_exp_f32 v202, v88\n\t"
      "v_exp_f32 v203, v89\n\t"
      "v_cvt_pk_bf16_f32 v115, v196, v197\n\t"
      "v_cvt_pk_bf16_f32 v123, v198, v199\n\t"
      "v_add_f32 v192, v192, v196\n\t"
      "v_add_f32 v193, v193, v197\n\t"
      "v_add_f32 v194, v194, v198\n\t"
      "v_add_f32 v195, v195, v199\n\t"
      "v_exp_f32 v196, v74\n\t"
      "v_exp_f32 v197, v75\n\t"
      "v_exp_f32 v198, v90\n\t"
      "v_exp_f32 v199, v91\n\t"
      "v_cvt_pk_bf16_f32 v116, v200, v201\n\t"
      "v_cvt_pk_bf16_f32 v124, v202, v203\n\t"
      "v_add_f32 v192, v192, v200\n\t"
      "v_add_f32 v193, v193, v201\n\t"
      "v_add_f32 v194, v194, v202\n\t"
      "v_add_f32 v195, v195, v203\n\t"
      "v_exp_f32 v200, v76\n\t"
      "v_exp_f32 v201, v77\n\t"
      "v_exp_f32 v202, v92\n\t"
      "v_exp_f32 v203, v93\n\t"
      "v_cvt_pk_bf16_f32 v117, v196, v197\n\t"
      "v_cvt_pk_bf16_f32 v125, v198, v199\n\t"
      "v_add_f32 v192, v192, v196\n\t"
      "v_add_f32 v193, v193, v197\n\t"
      "v_add_f32 v194, v194, v198\n\t"
      "v_add_f32 v195, v195, v199\n\t"
      "v_exp_f32 v196, v78\n\t"
      "v_exp_f32 v197, v79\n\t"
      "v_exp_f32 v198, v94\n\t"
      "v_exp_f32 v199, v95\n\t"
      "v_cvt_pk_bf16_f32 v118, v200, v201\n\t"
      "v_cvt_pk_bf16_f32 v126, v202, v203\n\t"
      "v_add_f32 v192, v192, v200\n\t"
      "v_add_f32 v193, v193, v201\n\t"
      "v_add_f32 v194, v194, v202\n\t"
      "v_add_f32 v195, v195, v203\n\t"
      "v_mov_b32 v211, v210\n\t"
      "v_add_f32 v192, v192, v196\n\t"
      "v_add_f32 v193, v193, v197\n\t"
      "v_add_f32 v194, v194, v198\n\t"
      "v_add_f32 v195, v195, v199\n\t"
      "v_add_f32 v192, v192, v194\n\t"
      "v_add_f32 v193, v193, v195\n\t"
      "v_add_f32 v192, v192, v193\n\t"
      "v_subrev_u32 v204, s58, v192\n\t"
      "v_cmp_lt_u32 vcc, s59, v204\n\t"
      "v_cvt_pk_bf16_f32 v119, v196, v197\n\t"
      "v_cvt_pk_bf16_f32 v127, v198, v199\n\t"
      "s_cbranch_vccnz .Lresc%=\n\t"
      "s_and_b32 s53, %[mq], s40\n\t"
      "s_cbranch_scc1 .Lqswap%=\n\t"
      ".Lret2%=:\n\t"
      "v_add_f32 v209, v209, v192\n\t"
      "v_mov_b32 v210, v212\n\t"
      "s_lshl_b32 s40, s40, 1\n\t"
      "s_mov_b32 s43, s44\n\t"
      "s_mov_b32 s44, s45\n\t"
      "s_mov_b32 s46, s47\n\t"
      "s_cmp_lg_u32 s40, 0x20000\n\t"
      "s_cbranch_scc1 .Lloop%=\n\t"
      "s_cmp_lg_u32 %[g], 0\n\t"
      "s_cbranch_scc1 .Lnob%=\n\t"
      "s_barrier\n\t"
      ".Lnob%=:\n\t"
      "s_waitcnt lgkmcnt(7)\n\t"
      "v_mfma_f32_32x32x16_bf16 v[0:15], v[160:163], v[112:115], v[0:15]\n\t"
      "ds_read_b128 v[160:163], v210 offset:4096\n\t"
      "s_waitcnt lgkmcnt(7)\n\t"
      "v_mfma_f32_32x32x16_bf16 v[0:15], v[164:167], v[120:123], v[0:15]\n\t"
      "ds_read_b128 v[164:167], v210 offset:12288\n\t"
      "s_waitcnt lgkmcnt(7)\n\t"
      "v_mfma_f32_32x32x16_bf16 v[16:31], v[168:171], v[112:115], v[16:31]\n\t"
      "ds_read_b128 v[168:171], v210 offset:5120\n\t"
      "s_waitcnt lgkmcnt(7)\n\t"
      "v_mfma_f32_32x32x16_bf16 v[16:31], v[172:175], v[120:123], v[16:31]\n\t"
      "ds_read_b128 v[172:175], v210 offset:13312\n\t"
      "s_waitcnt lgkmcnt(7)\n\t"
      "v_mfma_f32_32x32x16_bf16 v[32:47], v[176:179], v[112:115], v[32:47]\n\t"
      "ds_read_b128 v[176:179], v210 offset:6144\n\t"
      "s_waitcnt lgkmcnt(7)\n\t"
      "v_mfma_f32_32x32x16_bf16 v[32:47], v[180:183], v[120:123], v[32:47]\n\t"
      "ds_read_b128 v[180:183], v210 offset:14336\n\t"
      "s_waitcnt lgkmcnt(7)\n\t"
      "v_mfma_f32_32x32x16_bf16 v[48:63], v[184:187], v[112:115], v[48:63]\n\t"
      "ds_read_b128 v[184:187], v210 offset:7168\n\t"
      "s_waitcnt lgkmcnt(7)\n\t"
      "v_mfma_f32_32x32x16_bf16 v[48:63], v[188:191], v[120:123], v[48:63]\n\t"
      "ds_read_b128 v[188:191], v210 offset:15360\n\t"
      "s_waitcnt lgkmcnt(7)\n\t"
      "v_mfma_f32_32x32x16_bf16 v[0:15], v[160:163], v[116:119], v[0:15]\n\t"
      "s_waitcnt lgkmcnt(6)\n\t"
      "v_mfma_f32_32x32x16_bf16 v[0:15], v[164:167], v[124:127], v[0:15]\n\t"
      "s_waitcnt lgkmcnt(5)\n\t"
      "v_mfma_f32_32x32x16_bf16 v[16:31], v[168:171], v[116:119], v[16:31]\n\t"
      "s_waitcnt lgkmcnt(4)\n\t"
      "v_mfma_f32_32x32x16_bf16 v[16:31], v[172:175], v[124:127], v[16:31]\n\t"
      "s_waitcnt lgkmcnt(3)\n\t"
      "v_mfma_f32_32x32x16_bf16 v[32:47], v[176:179], v[116:119], v[32:47]\n\t"
      "s_waitcnt lgkmcnt(2)\n\t"
      "v_mfma_f32_32x32x16_bf16 v[32:47], v[180:183], v[124:127], v[32:47]\n\t"
      "s_waitcnt lgkmcnt(1)\n\t"
      "v_mfma_f32_32x32x16_bf16 v[48:63], v[184:187], v[116:119], v[48:63]\n\t"
      "s_waitcnt lgkmcnt(0)\n\t"
      "v_mfma_f32_32x32x16_bf16 v[48:63], v[188:191], v[124:127], v[48:63]\n\t"
      "s_nop 7\n\t"
      "s_waitcnt vmcnt(0)\n\t"
      "s_nop 7\n\t"
      "s_branch .Lend%=\n\t"
      ".Lspec%=:\n\t"
      "s_cmp_gt_u32 s40, 8\n\t"
      "s_cbranch_scc1 .Lsp1%=\n\t"
      "s_and_b32 s53, s40, 5\n\t"
      "s_cbranch_scc1 .Lfl%=\n\t"
      "s_cmp_eq_u32 s40, 2\n\t"
      "s_cselect_b32 s62, 2, 0xff\n\t"
      "s_mov_b32 s63, 0x40000\n\t"
      ".Lchk%=:\n\t"
      "v_cmp_ne_u32 vcc, %[tag], v225\n\t"
      "s_sub_u32 s63, s63, 1\n\t"
      "s_cbranch_scc1 .Lok%=\n\t"
      "s_and_b32 s53, vcc_lo, s62\n\t"
      "s_cbranch_scc0 .Lok%=\n\t"
      "buffer_load_dword v225, v226, %[sk], 0 offen sc1\n\t"
      "s_waitcnt vmcnt(0)\n\t"
      "s_branch .Lchk%=\n\t"
      ".Lok%=:\n\t"
      "s_branch .Lsp1%=\n\t"
      ".Lfl%=:\n\t"
      "buffer_load_dword v225, v226, %[sk], 0 offen sc1\n\t"
      "buffer_load_dwordx4 v[228:231], v227, %[sk], 0 offen sc1\n\t"
      ".Lsp1%=:\n\t"
      "s_cmp_lg_u32 s55, 0\n\t"
      "s_cbranch_scc1 .Lfr%=\n\t"
      "s_and_b32 s53, %[md], s40\n\t"
      "s_cbranch_scc0 .Lret%=\n\t"
      "s_branch .Lmask%=\n\t"
      ".Lfr%=:\n\t"
      "s_cmp_eq_u32 s40, 1\n\t"
      "s_cbranch_scc1 .Lfirst%=\n\t"
      "v_mov_b32 v204, v209\n\t"
      "v_mov_b32 v205, v209\n\t"
      "v_add_u32 v219, 0x1000, v216\n\t"
      "s_nop 0\n\t"
      "v_permlane32_swap_b32 v204, v205\n\t"
      "s_nop 1\n\t"
      "v_add_f32 v204, v204, v205\n\t"
      "v_rcp_f32 v204, v204\n\t"
      "s_nop 3\n\t"
      "v_mul_f32 v0, v0, v204\n\t"
      "v_mul_f32 v1, v1, v204\n\t"
      "v_mul_f32 v2, v2, v204\n\t"
      "v_mul_f32 v3, v3, v204\n\t"
      "v_mul_f32 v4, v4, v204\n\t"
      "v_mul_f32 v5, v5, v204\n\t"
      "v_mul_f32 v6, v6, v204\n\t"
      "v_mul_f32 v7, v7, v204\n\t"
      "v_cvt_pk_bf16_f32 v200, v0, v1\n\t"
      "v_cvt_pk_bf16_f32 v201, v2, v3\n\t"
      "v_cvt_pk_bf16_f32 v202, v4, v5\n\t"
      "v_cvt_pk_bf16_f32 v203, v6, v7\n\t"
      "buffer_store_dwordx4 v[200:203], v216, %[sc], 0 offen offset:0 sc1\n\t"
      "v_mul_f32 v8, v8, v204\n\t"
      "v_mul_f32 v9, v9, v204\n\t"
      "v_mul_f32 v10, v10, v204\n\t"
      "v_mul_f32 v11, v11, v204\n\t"
      "v_mul_f32 v12, v12, v204\n\t"
      "v_mul_f32 v13, v13, v204\n\t"
      "v_mul_f32 v14, v14, v204\n\t"
      "v_mul_f32 v15, v15, v204\n\t"
      "v_cvt_pk_bf16_f32 v196, v8, v9\n\t"
      "v_cvt_pk_bf16_f32 v197, v10, v11\n\t"
      "v_cvt_pk_bf16_f32 v198, v12, v13\n\t"
      "v_cvt_pk_bf16_f32 v199, v14, v15\n\t"
      "buffer_store_dwordx4 v[196:199], v216, %[sc], 0 offen offset:1024 sc1\n\t"
      "v_mul_f32 v16, v16, v204\n\t"
      "v_mul_f32 v17, v17, v204\n\t"
      "v_mul_f32 v18, v18, v204\n\t"
      "v_mul_f32 v19, v19, v204\n\t"
      "v_mul_f32 v20, v20, v204\n\t"
      "v_mul_f32 v21, v21, v204\n\t"
      "v_mul_f32 v22, v22, v204\n\t"
      "v_mul_f32 v23, v23, v204\n\t"
      "v_cvt_pk_bf16_f32 v200, v16, v17\n\t"
      "v_cvt_pk_bf16_f32 v201, v18, v19\n\t"
      "v_cvt_pk_bf16_f32 v202, v20, v21\n\t"
      "v_cvt_pk_bf16_f32 v203, v22, v23\n\t"
      "buffer_store_dwordx4 v[200:203], v216, %[sc], 0 offen offset:2048 sc1\n\t"
      "v_mul_f32 v24, v24, v204\n\t"
      "v_mul_f32 v25, v25, v204\n\t"
      "v_mul_f32 v26, v26, v204\n\t"
      "v_mul_f32 v27, v27, v204\n\t"
      "v_mul_f32 v28, v28, v204\n\t"
      "v_mul_f32 v29, v29, v204\n\t"
      "v_mul_f32 v30, v30, v204\n\t"
      "v_mul_f32 v31, v31, v204\n\t"
      "v_cvt_pk_bf16_f32 v196, v24, v25\n\t"
      "v_cvt_pk_bf16_f32 v197, v26, v27\n\t"
      "v_cvt_pk_bf16_f32 v198, v28, v29\n\t"
      "v_cvt_pk_bf16_f32 v199, v30, v31\n\t"
      "buffer_store_dwordx4 v[196:199], v216, %[sc], 0 offen offset:3072 sc1\n\t"
      "v_mul_f32 v32, v32, v204\n\t"
      "v_mul_f32 v33, v33, v204\n\t"
      "v_mul_f32 v34, v34, v204\n\t"
      "v_mul_f32 v35, v35, v204\n\t"
      "v_mul_f32 v36, v36, v204\n\t"
      "v_mul_f32 v37, v37, v204\n\t"
      "v_mul_f32 v38, v38, v204\n\t"
      "v_mul_f32 v39, v39, v204\n\t"
      "v_cvt_pk_bf16_f32 v200, v32, v33\n\t"
      "v_cvt_pk_bf16_f32 v201, v34, v35\n\t"
      "v_cvt_pk_bf16_f32 v202, v36, v37\n\t"
      "v_cvt_pk_bf16_f32 v203, v38, v39\n\t"
      "buffer_store_dwordx4 v[200:203], v219, %[sc], 0 offen offset:0 sc1\n\t"
      "v_mul_f32 v40, v40, v204\n\t"
      "v_mul_f32 v41, v41, v204\n\t"
      "v_mul_f32 v42, v42, v204\n\t"
      "v_mul_f32 v43, v43, v204\n\t"
      "v_mul_f32 v44, v44, v204\n\t"
      "v_mul_f32 v45, v45, v204\n\t"
      "v_mul_f32 v46, v46, v204\n\t"
      "v_mul_f32 v47, v47, v204\n\t"
      "v_cvt_pk_bf16_f32 v196, v40, v41\n\t"
      "v_cvt_pk_bf16_f32 v197, v42, v43\n\t"
      "v_cvt_pk_bf16_f32 v198, v44, v45\n\t"
      "v_cvt_pk_bf16_f32 v199, v46, v47\n\t"
      "buffer_store_dwordx4 v[196:199], v219, %[sc], 0 offen offset:1024 sc1\n\t"
      "v_mul_f32 v48, v48, v204\n\t"
      "v_mul_f32 v49, v49, v204\n\t"
      "v_mul_f32 v50, v50, v204\n\t"
      "v_mul_f32 v51, v51, v204\n\t"
      "v_mul_f32 v52, v52, v204\n\t"
      "v_mul_f32 v53, v53, v204\n\t"
      "v_mul_f32 v54, v54, v204\n\t"
      "v_mul_f32 v55, v55, v204\n\t"
      "v_cvt_pk_bf16_f32 v200, v48, v49\n\t"
      "v_cvt_pk_bf16_f32 v201, v50, v51\n\t"
      "v_cvt_pk_bf16_f32 v202, v52, v53\n\t"
      "v_cvt_pk_bf16_f32 v203, v54, v55\n\t"
      "buffer_store_dwordx4 v[200:203], v219, %[sc], 0 offen offset:2048 sc1\n\t"
      "v_mul_f32 v56, v56, v204\n\t"
      "v_mul_f32 v57, v57, v204\n\t"
      "v_mul_f32 v58, v58, v204\n\t"
      "v_mul_f32 v59, v59, v204\n\t"
      "v_mul_f32 v60, v60, v204\n\t"
      "v_mul_f32 v61, v61, v204\n\t"
      "v_mul_f32 v62, v62, v204\n\t"
      "v_mul_f32 v63, v63, v204\n\t"
      "v_cvt_pk_bf16_f32 v196, v56, v57\n\t"
      "v_cvt_pk_bf16_f32 v197, v58, v59\n\t"
      "v_cvt_pk_bf16_f32 v198, v60, v61\n\t"
      "v_cvt_pk_bf16_f32 v199, v62, v63\n\t"
      "buffer_store_dwordx4 v[196:199], v219, %[sc], 0 offen offset:3072 sc1\n\t"
      "s_branch .Lzero%=\n\t"
      ".Lfirst%=:\n\t"
      "v_cmp_gt_i32_e64 s[64:65], 0, v215\n\t"
      "v_cmp_gt_i32_e64 s[66:67], 1, v215\n\t"
      "v_cmp_gt_i32_e64 s[68:69], 2, v215\n\t"
      "v_cmp_gt_i32_e64 s[70:71], 3, v215\n\t"
      "v_cmp_gt_i32_e64 s[72:73], 8, v215\n\t"
      "v_cmp_gt_i32_e64 s[74:75], 9, v215\n\t"
      "v_cmp_gt_i32_e64 s[76:77], 10, v215\n\t"
      "v_cmp_gt_i32_e64 s[78:79], 11, v215\n\t"
      "v_cmp_gt_i32_e64 s[80:81], 16, v215\n\t"
      "v_cmp_gt_i32_e64 s[82:83], 17, v215\n\t"
      "v_cmp_gt_i32_e64 s[84:85], 18, v215\n\t"
      "v_cmp_gt_i32_e64 s[86:87], 19, v215\n\t"
      "v_cmp_gt_i32_e64 s[88:89], 24, v215\n\t"
      "v_cmp_gt_i32_e64 s[90:91], 25, v215\n\t"
      "v_cmp_gt_i32_e64 s[92:93], 26, v215\n\t"
      "v_cmp_gt_i32_e64 s[94:95], 27, v215\n\t"
      "v_mov_b32 v218, 0xff800000\n\t"
      ".Lzero%=:\n\t"
      "v_mov_b64 v[0:1], 0\n\t"
      "v_mov_b64 v[2:3], 0\n\t"
      "v_mov_b64 v[4:5], 0\n\t"
      "v_mov_b64 v[6:7], 0\n\t"
      "v_mov_b64 v[8:9], 0\n\t"
      "v_mov_b64 v[10:11], 0\n\t"
      "v_mov_b64 v[12:13], 0\n\t"
      "v_mov_b64 v[14:15], 0\n\t"
      "v_mov_b64 v[16:17], 0\n\t"
      "v_mov_b64 v[18:19], 0\n\t"
      "v_mov_b64 v[20:21], 0\n\t"
      "v_mov_b64 v[22:23], 0\n\t"
      "v_mov_b64 v[24:25], 0\n\t"
      "v_mov_b64 v[26:27], 0\n\t"
      "v_mov_b64 v[28:29], 0\n\t"
      "v_mov_b64 v[30:31], 0\n\t"
      "v_mov_b64 v[32:33], 0\n\t"
      "v_mov_b64 v[34:35], 0\n\t"
      "v_mov_b64 v[36:37], 0\n\t"
      "v_mov_b64 v[38:39], 0\n\t"
      "v_mov_b64 v[40:41], 0\n\t"
      "v_mov_b64 v[42:43], 0\n\t"
      "v_mov_b64 v[44:45], 0\n\t"
      "v_mov_b64 v[46:47], 0\n\t"
      "v_mov_b64 v[48:49], 0\n\t"
      "v_mov_b64 v[50:51], 0\n\t"
      "v_mov_b64 v[52:53], 0\n\t"
      "v_mov_b64 v[54:55], 0\n\t"
      "v_mov_b64 v[56:57], 0\n\t"
      "v_mov_b64 v[58:59], 0\n\t"
      "v_mov_b64 v[60:61], 0\n\t"
      "v_mov_b64 v[62:63], 0\n\t"
      "v_mov_b32 v208, 0\n\t"
      "v_mov_b32 v209, 0\n\t"
      "s_and_b32 s53, %[md], s40\n\t"
      "s_cbranch_scc0 .Lret%=\n\t"
      ".Lmask%=:\n\t"
      "s_and_b32 s53, s43, 0x8000\n\t"
      "s_lshr_b32 s53, s53, 14\n\t"
      "s_sub_i32 s53, %[qq], s53\n\t"
      "s_cmp_ge_i32 s53, 2\n\t"
      "s_cbranch_scc1 .Lret%=\n\t"
      "s_cmp_eq_u32 s53, 1\n\t"
      "s_cbranch_scc0 .Lm2%=\n\t"
      "v_cndmask_b32_e64 v80, v80, v218, s[64:65]\n\t"
      "v_cndmask_b32_e64 v81, v81, v218, s[66:67]\n\t"
      "v_cndmask_b32_e64 v82, v82, v218, s[68:69]\n\t"
      "v_cndmask_b32_e64 v83, v83, v218, s[70:71]\n\t"
      "v_cndmask_b32_e64 v84, v84, v218, s[72:73]\n\t"
      "v_cndmask_b32_e64 v85, v85, v218, s[74:75]\n\t"
      "v_cndmask_b32_e64 v86, v86, v218, s[76:77]\n\t"
      "v_cndmask_b32_e64 v87, v87, v218, s[78:79]\n\t"
      "v_cndmask_b32_e64 v88, v88, v218, s[80:81]\n\t"
      "v_cndmask_b32_e64 v89, v89, v218, s[82:83]\n\t"
      "v_cndmask_b32_e64 v90, v90, v218, s[84:85]\n\t"
      "v_cndmask_b32_e64 v91, v91, v218, s[86:87]\n\t"
      "v_cndmask_b32_e64 v92, v92, v218, s[88:89]\n\t"
      "v_cndmask_b32_e64 v93, v93, v218, s[90:91]\n\t"
      "v_cndmask_b32_e64 v94, v94, v218, s[92:93]\n\t"
      "v_cndmask_b32_e64 v95, v95, v218, s[94:95]\n\t"
      "s_branch .Lret%=\n\t"
      ".Lm2%=:\n\t"
      "v_mov_b32 v80, v218\n\t"
      "v_mov_b32 v81, v218\n\t"
      "v_mov_b32 v82, v218\n\t"
      "v_mov_b32 v83, v218\n\t"
      "v_mov_b32 v84, v218\n\t"
      "v_mov_b32 v85, v218\n\t"
      "v_mov_b32 v86, v218\n\t"
      "v_mov_b32 v87, v218\n\t"
      "v_mov_b32 v88, v218\n\t"
      "v_mov_b32 v89, v218\n\t"
      "v_mov_b32 v90, v218\n\t"
      "v_mov_b32 v91, v218\n\t"
      "v_mov_b32 v92, v218\n\t"
      "v_mov_b32 v93, v218\n\t"
      "v_mov_b32 v94, v218\n\t"
      "v_mov_b32 v95, v218\n\t"
      "s_cmp_eq_u32 s53, 0\n\t"
      "s_cbranch_scc0 .Lm3%=\n\t"
      "v_cndmask_b32_e64 v64, v64, v218, s[64:65]\n\t"
      "v_cndmask_b32_e64 v65, v65, v218, s[66:67]\n\t"
      "v_cndmask_b32_e64 v66, v66, v218, s[68:69]\n\t"
      "v_cndmask_b32_e64 v67, v67, v218, s[70:71]\n\t"
      "v_cndmask_b32_e64 v68, v68, v218, s[72:73]\n\t"
      "v_cndmask_b32_e64 v69, v69, v218, s[74:75]\n\t"
      "v_cndmask_b32_e64 v70, v70, v218, s[76:77]\n\t"
      "v_cndmask_b32_e64 v71, v71, v218, s[78:79]\n\t"
      "v_cndmask_b32_e64 v72, v72, v218, s[80:81]\n\t"
      "v_cndmask_b32_e64 v73, v73, v218, s[82:83]\n\t"
      "v_cndmask_b32_e64 v74, v74, v218, s[84:85]\n\t"
      "v_cndmask_b32_e64 v75, v75, v218, s[86:87]\n\t"
      "v_cndmask_b32_e64 v76, v76, v218, s[88:89]\n\t"
      "v_cndmask_b32_e64 v77, v77, v218, s[90:91]\n\t"
      "v_cndmask_b32_e64 v78, v78, v218, s[92:93]\n\t"
      "v_cndmask_b32_e64 v79, v79, v218, s[94:95]\n\t"
      "s_branch .Lret%=\n\t"
      ".Lm3%=:\n\t"
      "v_mov_b32 v64, v218\n\t"
      "v_mov_b32 v65, v218\n\t"
      "v_mov_b32 v66, v218\n\t"
      "v_mov_b32 v67, v218\n\t"
      "v_mov_b32 v68, v218\n\t"
      "v_mov_b32 v69, v218\n\t"
      "v_mov_b32 v70, v218\n\t"
      "v_mov_b32 v71, v218\n\t"
      "v_mov_b32 v72, v218\n\t"
      "v_mov_b32 v73, v218\n\t"
      "v_mov_b32 v74, v218\n\t"
      "v_mov_b32 v75, v218\n\t"
      "v_mov_b32 v76, v218\n\t"
      "v_mov_b32 v77, v218\n\t"
      "v_mov_b32 v78, v218\n\t"
      "v_mov_b32 v79, v218\n\t"
      "s_branch .Lret%=\n\t"
      ".Lresc%=:\n\t"
      "v_max3_f32 v204, v64, v65, v66\n\t"
      "v_max3_f32 v205, v67, v68, v69\n\t"
      "v_max3_f32 v204, v204, v70, v71\n\t"
      "v_max3_f32 v205, v205, v72, v73\n\t"
      "v_max3_f32 v204, v204, v74, v75\n\t"
      "v_max3_f32 v205, v205, v76, v77\n\t"
      "v_max3_f32 v204, v204, v78, v79\n\t"
      "v_max3_f32 v205, v205, v80, v81\n\t"
      "v_max3_f32 v204, v204, v82, v83\n\t"
      "v_max3_f32 v205, v205, v84, v85\n\t"
      "v_max3_f32 v204, v204, v86, v87\n\t"
      "v_max3_f32 v205, v205, v88, v89\n\t"
      "v_max3_f32 v204, v204, v90, v91\n\t"
      "v_max3_f32 v205, v205, v92, v93\n\t"
      "v_max3_f32 v204, v204, v94, v95\n\t"
      "v_max_f32 v204, v204, v205\n\t"
      "s_mov_b32 s59, -1\n\t"
      "v_mov_b32 v205, v204\n\t"
      "s_nop 1\n\t"
      "v_permlane32_swap_b32 v204, v205\n\t"
      "s_nop 1\n\t"
      "v_max3_f32 v204, v204, v205, s56\n\t"
      "v_add_f32 v208, v208, v204\n\t"
      "s_cmp_lg_u32 s55, 0\n\t"
      "s_cbranch_scc1 .Lrc%=\n\t"
      "v_exp_f32 v206, -v204\n\t"
      "s_nop 1\n\t"
      "v_mul_f32 v209, v209, v206\n\t"
      "v_mul_f32 v0, v0, v206\n\t"
      "v_mul_f32 v1, v1, v206\n\t"
      "v_mul_f32 v2, v2, v206\n\t"
      "v_mul_f32 v3, v3, v206\n\t"
      "v_mul_f32 v4, v4, v206\n\t"
      "v_mul_f32 v5, v5, v206\n\t"
      "v_mul_f32 v6, v6, v206\n\t"
      "v_mul_f32 v7, v7, v206\n\t"
      "v_mul_f32 v8, v8, v206\n\t"
      "v_mul_f32 v9, v9, v206\n\t"
      "v_mul_f32 v10, v10, v206\n\t"
      "v_mul_f32 v11, v11, v206\n\t"
      "v_mul_f32 v12, v12, v206\n\t"
      "v_mul_f32 v13, v13, v206\n\t"
      "v_mul_f32 v14, v14, v206\n\t"
      "v_mul_f32 v15, v15, v206\n\t"
      "v_mul_f32 v16, v16, v206\n\t"
      "v_mul_f32 v17, v17, v206\n\t"
      "v_mul_f32 v18, v18, v206\n\t"
      "v_mul_f32 v19, v19, v206\n\t"
      "v_mul_f32 v20, v20, v206\n\t"
      "v_mul_f32 v21, v21, v206\n\t"
      "v_mul_f32 v22, v22, v206\n\t"
      "v_mul_f32 v23, v23, v206\n\t"
      "v_mul_f32 v24, v24, v206\n\t"
      "v_mul_f32 v25, v25, v206\n\t"
      "v_mul_f32 v26, v26, v206\n\t"
      "v_mul_f32 v27, v27, v206\n\t"
      "v_mul_f32 v28, v28, v206\n\t"
      "v_mul_f32 v29, v29, v206\n\t"
      "v_mul_f32 v30, v30, v206\n\t"
      "v_mul_f32 v31, v31, v206\n\t"
      "v_mul_f32 v32, v32, v206\n\t"
      "v_mul_f32 v33, v33, v206\n\t"
      "v_mul_f32 v34, v34, v206\n\t"
      "v_mul_f32 v35, v35, v206\n\t"
      "v_mul_f32 v36, v36, v206\n\t"
      "v_mul_f32 v37, v37, v206\n\t"
      "v_mul_f32 v38, v38, v206\n\t"
      "v_mul_f32 v39, v39, v206\n\t"
      "v_mul_f32 v40, v40, v206\n\t"
      "v_mul_f32 v41, v41, v206\n\t"
      "v_mul_f32 v42, v42, v206\n\t"
      "v_mul_f32 v43, v43, v206\n\t"
      "v_mul_f32 v44, v44, v206\n\t"
      "v_mul_f32 v45, v45, v206\n\t"
      "v_mul_f32 v46, v46, v206\n\t"
      "v_mul_f32 v47, v47, v206\n\t"
      "v_mul_f32 v48, v48, v206\n\t"
      "v_mul_f32 v49, v49, v206\n\t"
      "v_mul_f32 v50, v50, v206\n\t"
      "v_mul_f32 v51, v51, v206\n\t"
      "v_mul_f32 v52, v52, v206\n\t"
      "v_mul_f32 v53, v53, v206\n\t"
      "v_mul_f32 v54, v54, v206\n\t"
      "v_mul_f32 v55, v55, v206\n\t"
      "v_mul_f32 v56, v56, v206\n\t"
      "v_mul_f32 v57, v57, v206\n\t"
      "v_mul_f32 v58, v58, v206\n\t"
      "v_mul_f32 v59, v59, v206\n\t"
      "v_mul_f32 v60, v60, v206\n\t"
      "v_mul_f32 v61, v61, v206\n\t"
      "v_mul_f32 v62, v62, v206\n\t"
      "v_mul_f32 v63, v63, v206\n\t"
      ".Lrc%=:\n\t"
      "v_sub_f32 v64, v64, v204\n\t"
      "v_sub_f32 v65, v65, v204\n\t"
      "v_sub_f32 v66, v66, v204\n\t"
      "v_sub_f32 v67, v67, v204\n\t"
      "v_sub_f32 v68, v68, v204\n\t"
      "v_sub_f32 v69, v69, v204\n\t"
      "v_sub_f32 v70, v70, v204\n\t"
      "v_sub_f32 v71, v71, v204\n\t"
      "v_sub_f32 v72, v72, v204\n\t"
      "v_sub_f32 v73, v73, v204\n\t"
      "v_sub_f32 v74, v74, v204\n\t"
      "v_sub_f32 v75, v75, v204\n\t"
      "v_sub_f32 v76, v76, v204\n\t"
      "v_sub_f32 v77, v77, v204\n\t"
      "v_sub_f32 v78, v78, v204\n\t"
      "v_sub_f32 v79, v79, v204\n\t"
      "v_sub_f32 v80, v80, v204\n\t"
      "v_sub_f32 v81, v81, v204\n\t"
      "v_sub_f32 v82, v82, v204\n\t"
      "v_sub_f32 v83, v83, v204\n\t"
      "v_sub_f32 v84, v84, v204\n\t"
      "v_sub_f32 v85, v85, v204\n\t"
      "v_sub_f32 v86, v86, v204\n\t"
      "v_sub_f32 v87, v87, v204\n\t"
      "v_sub_f32 v88, v88, v204\n\t"
      "v_sub_f32 v89, v89, v204\n\t"
      "v_sub_f32 v90, v90, v204\n\t"
      "v_sub_f32 v91, v91, v204\n\t"
      "v_sub_f32 v92, v92, v204\n\t"
      "v_sub_f32 v93, v93, v204\n\t"
      "v_sub_f32 v94, v94, v204\n\t"
      "v_sub_f32 v95, v95, v204\n\t"
      "v_xor_b32 v96, 0x80000000, v208\n\t"
      "v_xor_b32 v97, 0x80000000, v208\n\t"
      "v_mov_b64 v[98:99], v[96:97]\n\t"
      "v_mov_b64 v[100:101], v[96:97]\n\t"
      "v_mov_b64 v[102:103], v[96:97]\n\t"
      "v_mov_b64 v[104:105], v[96:97]\n\t"
      "v_mov_b64 v[106:107], v[96:97]\n\t"
      "v_mov_b64 v[108:109], v[96:97]\n\t"
      "v_mov_b64 v[110:111], v[96:97]\n\t"
      "s_branch .Lret%=\n\t"
      ".Lqswap%=:\n\t"
      "ds_read_b128 v[128:131], v217 offset:0\n\t"
      "ds_read_b128 v[132:135], v217 offset:1024\n\t"
      "ds_read_b128 v[136:139], v217 offset:2048\n\t"
      "ds_read_b128 v[140:143], v217 offset:3072\n\t"
      "ds_read_b128 v[144:147], v217 offset:4096\n\t"
      "ds_read_b128 v[148:151], v217 offset:5120\n\t"
      "ds_read_b128 v[152:155], v217 offset:6144\n\t"
      "ds_read_b128 v[156:159], v217 offset:7168\n\t"
      "v_mov_b64 v[96:97], 0\n\t"
      "v_mov_b64 v[98:99], 0\n\t"
      "v_mov_b64 v[100:101], 0\n\t"
      "v_mov_b64 v[102:103], 0\n\t"
      "v_mov_b64 v[104:105], 0\n\t"
      "v_mov_b64 v[106:107], 0\n\t"
      "v_mov_b64 v[108:109], 0\n\t"
      "v_mov_b64 v[110:111], 0\n\t"
      "s_waitcnt lgkmcnt(0)\n\t"
      "s_branch .Lret2%=\n\t"
      ".Lend%=:\n\t"
      : "={v[0:15]}"(O[0]), "={v[16:31]}"(O[1]), "={v[32:47]}"(O[2]), "={v[48:63]}"(O[3]), "={v208}"(m), "={v209}"(l), "+{v[128:143]}"(Q0), "+{v[144:159]}"(Q1), "={v[228:231]}"(hf4)
      : "{v213}"(l16 + qq * 4096u), "{v214}"(ldsb + l16), "{v215}"(l31 - 4 * h), "{v216}"(l16 + (unsigned)((j * 4 + qq) * 256 + head * 8) * 1024u),
        "{v217}"(ldsb + l16 + 131072u + qq * 8192u), "{v224}"(pfo), "{v226}"(KFL + head * 128u + (lane & 7) * 4u), "{v227}"(tid < 64 && b == 0 ? KFL + 4096u + l16 : 0x40000000u), [tag] "s"(tag), [spf] "s"(spf), [sk] "s"(sk), [sv] "s"(sv), [sc] "s"(sc), [dbase] "s"(ldsb + qq * 4096u), [hb] "s"(hb), [mk] "s"(mK), [mv] "s"(mV),
        [md] "s"(mD), [mf] "s"(mF), [mq] "s"(mQ), [y2] "s"(4 - p + g), [g] "s"(g), [dum] "s"(ldsb + 0x30000u), [qq] "s"(qq), [cpp] "s"(cpp), [cg] "s"(cgo), [cm] "s"(cm)
      : "memory", "vcc", "scc", CLOB_V, CLOB_S, "v232", "v233", "v234", "v235");
  STAMP(54);
  { auto rr = __builtin_amdgcn_permlane32_swap(__float_as_uint(l), __float_as_uint(l), false, false); l = __uint_as_float(rr[0]) + __uint_as_float(rr[1]); }
  const unsigned xb = (j & 1) ? 65536u : 0u;
  {
    LAS f32x4* X = (LAS f32x4*)((lds_ptr)lds + xb + g * 32768 + qq * 8192);
    LAS float* ML = (LAS float*)((lds_ptr)lds + 131072) + w * 128;
    ML[lane] = m; ML[64 + lane] = l;
    if (g == 0) {
#pragma unroll
      for (int d = 0; d < 2; ++d)
#pragma unroll
        for (int e = 0; e < 4; ++e) X[(d * 4 + e) * 64 + lane] = (f32x4){O[2 + d][4 * e], O[2 + d][4 * e + 1], O[2 + d][4 * e + 2], O[2 + d][4 * e + 3]};
    } else {
#pragma unroll
      for (int d = 0; d < 2; ++d)
#pragma unroll
        for (int e = 0; e < 4; ++e) X[(d * 4 + e) * 64 + lane] = (f32x4){O[d][4 * e], O[d][4 * e + 1], O[d][4 * e + 2], O[d][4 * e + 3]};
      O[0] = O[2]; O[1] = O[3];
    }
  }
  WAITBAR("lgkmcnt(0)");
  STAMP(55);
  {
    const LAS f32x4* Y = (const LAS f32x4*)((lds_ptr)lds + xb + (g ^ 1) * 32768 + qq * 8192);
    const LAS float* ML2 = (const LAS float*)((lds_ptr)lds + 131072) + (w ^ 4) * 128;
    const float m2 = ML2[lane], l2 = ML2[64 + lane];
    const float mn = __builtin_fmaxf(m, m2);
    float fa = __builtin_amdgcn_exp2f(m - mn), fb = __builtin_amdgcn_exp2f(m2 - mn);
    const float inv = __builtin_amdgcn_rcpf(l * fa + l2 * fb);
    fa *= inv; fb *= inv;
    const unsigned so = l16 + (unsigned)(((15 - j) * 4 + qq) * 256 + head * 8 + g * 4) * 1024u;
#pragma unroll
    for (int d = 0; d < 2; ++d)
#pragma unroll
      for (int sp = 0; sp < 2; ++sp) {
        const f32x4 y0 = Y[(d * 4 + 2 * sp) * 64 + lane], y1 = Y[(d * 4 + 2 * sp + 1) * 64 + lane];
        float o[8];
#pragma unroll
        for (int e = 0; e < 4; ++e) { o[e] = O[d][8 * sp + e] * fa + y0[e] * fb; o[4 + e] = O[d][8 * sp + 4 + e] * fa + y1[e] * fb; }
        u32x4 ov = {cvtpk(o[0], o[1]), cvtpk(o[2], o[3]), cvtpk(o[4], o[5]), cvtpk(o[6], o[7])};
        __builtin_amdgcn_raw_buffer_store_b128(ov, sc, so + (d * 2 + sp) * 1024u, 0, AUXC);
      }
  }
  if (b == 0 && w == 0) {
#pragma unroll 1
    for (int spin = 0; spin < SPIN; ++spin) {
      if (__all(hf4[0] == tag && hf4[1] == tag && hf4[2] == tag && hf4[3] == tag)) break;
      asm volatile("buffer_load_dwordx4 %0, %1, %2, 0 offen sc1\n\ts_waitcnt vmcnt(0)" : "=v"(hf4) : "v"(KFL + 4096u + l16), "s"(sk) : "memory");
    }
    __builtin_amdgcn_raw_buffer_store_b32(tag, mk_srd(ws, 32768u), lane == 0 ? (unsigned)WS_EP : 0x40000000u, 0, 16);
  }
  if (PFC) asm volatile(".Lkva_tail:");
  STAMP(56);
  STAMP_OUT(0, 8);
}

extern "C" void kernel_launch(void* const* d_in, const int* in_sizes, int n_in, void* d_out, int out_size, void* d_ws, size_t ws_size, hipStream_t stream) {
  (void)in_sizes; (void)n_in; (void)out_size; (void)ws_size;
  const u16* x = (const u16*)d_in[0]; const u16* ckv = (const u16*)d_in[1];
  const u16* wq = (const u16*)d_in[2]; const u16* wuk = (const u16*)d_in[3]; const u16* wuv = (const u16*)d_in[4]; const u16* wo = (const u16*)d_in[5];
  const u16* wg = (const u16*)d_in[7]; const u16* wu = (const u16*)d_in[8]; const u16* wd = (const u16*)d_in[9];
  const u16* c = (const u16*)d_in[10]; const u16* wmod = (const u16*)d_in[11];
  char* ws = (char*)d_ws + (WSEND ? ((ws_size - WS_END) & ~(size_t)0x1fffff) : 0);
  static bool attr = false;
  if (!attr) {
    attr = true;
    (void)hipFuncSetAttribute((const void*)k_kva, hipFuncAttributeMaxDynamicSharedMemorySize, 163840);
    (void)hipFuncSetAttribute((const void*)k_tail, hipFuncAttributeMaxDynamicSharedMemorySize, 163840);
  }
  k_kva<<<256, 512, 163840, stream>>>(ckv, wuk, wuv, x, c, wmod, wq, wo, wg, wu, wd, ws PROF_PASS);
  k_tail<<<256, 512, 163840, stream>>>(x, wo, wg, wu, wd, ws, (u16*)d_out PROF_PASS);
}
```
